```python
import torch

def _softmax(x, axis=-1):
    return torch.softmax(x, dim=axis)
def _gelu(x):
    return torch.nn.functional.gelu(x, approximate="tanh")
def _silu(x):
    return torch.nn.functional.silu(x)
def _sigmoid(x):
    return torch.sigmoid(x)
def _layernorm(x, axis=-1, eps=1e-5):
    m = x.mean(dim=axis, keepdim=True)
    v = x.var(dim=axis, keepdim=True, unbiased=False)
    return (x - m) / torch.sqrt(v + eps)
def _scatter_add(tgt, idx, src, axis=0):
    return tgt.clone().index_add_(axis, idx, src)
def _segment_sum(src, seg, n):
    out = torch.zeros((n,) + src.shape[1:], dtype=src.dtype)
    return out.index_add_(0, seg, src)
def _pad(x, pw):
    flat = tuple(v for pair in reversed(pw) for v in pair)
    return torch.nn.functional.pad(x, flat)

kernel_name = 'interleave_layers__mha_block_cached__glu_gated_geglu__decode'

def setup_inputs():

    _w = (lambda a: (lambda t: t.to(torch.bfloat16) if t.is_floating_point() else t)(torch.as_tensor(a)))
    def _A_setup():
        B, Sq, D = 4, 1, 640
        H, Sk, Dh = 32, 8192, 192
        return {
            "x": _w((lambda *s: torch.randn(*s, dtype=torch.bfloat16) * 0.05)(B, Sq, D)),
            "k_cache": _w((lambda *s: torch.randn(*s, dtype=torch.bfloat16) * 0.4)(B, H, Sk, Dh)),
            "v_cache": _w((lambda *s: torch.randn(*s, dtype=torch.bfloat16) * 1.6)(B, H, Sk, Dh)),
            "wq": _w((lambda *s: torch.randn(*s, dtype=torch.bfloat16) * 0.4)(D, H * Dh) * (D ** -0.5)),
            "wo": _w((lambda *s: torch.randn(*s, dtype=torch.bfloat16) * 1.6)(H * Dh, D) * ((H * Dh) ** -0.5)),
        }
    _A_in = _A_setup()

    def _B_setup():
        B, S, Dm, Df = 4, 1, 640, 1536
        return {
            "x": _w((lambda *s: torch.randn(*s, dtype=torch.bfloat16) * 0.05)(B, S, Dm)),
            "w_gate": _w((lambda *s: torch.randn(*s, dtype=torch.bfloat16) * 0.4)(Dm, Df) * (Dm ** -0.5)),
            "w_up": _w((lambda *s: torch.randn(*s, dtype=torch.bfloat16) * 0.4)(Dm, Df) * (Dm ** -0.5)),
            "w_down": _w((lambda *s: torch.randn(*s, dtype=torch.bfloat16) * 0.4)(Df, Dm) * (Df ** -0.5)),
        }
    _B_in = _B_setup()

    return {
        "A_x": _A_in["x"],
        "A_k_cache": _A_in["k_cache"],
        "A_v_cache": _A_in["v_cache"],
        "A_wq": _A_in["wq"],
        "A_wo": _A_in["wo"],
        "B_x": _B_in["x"],
        "B_w_gate": _B_in["w_gate"],
        "B_w_up": _B_in["w_up"],
        "B_w_down": _B_in["w_down"],
    }

def reference(A_x, A_k_cache, A_v_cache, A_wq, A_wo, B_x, B_w_gate, B_w_up, B_w_down):
    def _A_ref(x, k_cache, v_cache, wq, wo):
        B, Sq, D = x.shape
        H, Dh = 32, 192
        q = (lambda x, *d: x.permute(*d))((x @ wq).reshape(B, Sq, H, Dh), 0, 2, 1, 3)
        scores = torch.einsum("bhqd,bhkd->bhqk", q, k_cache) * (Dh ** -0.5)
        p = _softmax(scores, axis=-1)
        o = torch.einsum("bhqk,bhkd->bhqd", p, v_cache)
        return (lambda x, *d: x.permute(*d))(o, 0, 2, 1, 3).reshape(B, Sq, H * Dh) @ wo

    def _B_ref(x, w_gate, w_up, w_down):
        g = torch.einsum("bsd,df->bsf", x, w_gate)
        u = torch.einsum("bsd,df->bsf", x, w_up)
        h = _gelu(g) * u
        return torch.einsum("bsf,fd->bsd", h, w_down)

    x = A_x
    x = x + _A_ref(_layernorm(x, axis=-1), A_k_cache, A_v_cache, A_wq, A_wo)
    x = x + _B_ref(_layernorm(x, axis=-1), B_w_gate, B_w_up, B_w_down)
    x = x + _A_ref(_layernorm(x, axis=-1), A_k_cache, A_v_cache, A_wq, A_wo)
    x = x + _B_ref(_layernorm(x, axis=-1), B_w_gate, B_w_up, B_w_down)
    return x
```

```cpp
#include <hip/hip_runtime.h>
#include <hip/hip_bf16.h>
#include <stdint.h>

typedef unsigned short bf16_t;
typedef unsigned v4u __attribute__((ext_vector_type(4)));
typedef float f32x4 __attribute__((ext_vector_type(4)));
typedef __bf16 bf16x2 __attribute__((ext_vector_type(2)));

constexpr int B = 4, D = 640, H = 32, SK = 8192, DH = 192, DF = 1536, HD = H * DH;
constexpr int NSPLIT = 8;

__device__ __forceinline__ float bf2f(unsigned short b) { return __builtin_bit_cast(float, (unsigned)b << 16); }
__device__ __forceinline__ unsigned short f2bf(float f) { unsigned u = __builtin_bit_cast(unsigned, f); u += 0x7FFFu + ((u >> 16) & 1u); return (unsigned short)(u >> 16); }
__device__ __forceinline__ float dot2(unsigned k, unsigned q, float acc) {
  return __builtin_amdgcn_fdot2_f32_bf16(__builtin_bit_cast(bf16x2, k), __builtin_bit_cast(bf16x2, q), acc, false);
}
__device__ __forceinline__ float wave_sum(float v) { for (int o = 32; o > 0; o >>= 1) v += __shfl_xor(v, o); return v; }
__device__ __forceinline__ float wave_max(float v) { for (int o = 32; o > 0; o >>= 1) v = fmaxf(v, __shfl_xor(v, o)); return v; }

__device__ void ln_row(const float* x, float* y, float* red) {
  int tid = threadIdx.x;
  float s = 0.f;
  for (int i = tid; i < D; i += blockDim.x) s += x[i];
  s = wave_sum(s);
  if ((tid & 63) == 0) red[tid >> 6] = s;
  __syncthreads();
  float tot = 0.f; for (int w = 0; w < (int)blockDim.x / 64; ++w) tot += red[w];
  float mean = tot / D;
  __syncthreads();
  float v = 0.f;
  for (int i = tid; i < D; i += blockDim.x) { float d = x[i] - mean; v += d * d; }
  v = wave_sum(v);
  if ((tid & 63) == 0) red[tid >> 6] = v;
  __syncthreads();
  float tv = 0.f; for (int w = 0; w < (int)blockDim.x / 64; ++w) tv += red[w];
  float rs = 1.0f / sqrtf(tv / D + 1e-5f);
  for (int i = tid; i < D; i += blockDim.x) y[i] = (x[i] - mean) * rs;
  __syncthreads();
}

__global__ void k_init(const bf16_t* x, float* xf) { int i = blockIdx.x * blockDim.x + threadIdx.x; if (i < B * D) xf[i] = bf2f(x[i]); }

__global__ void __launch_bounds__(256) k_q(const float* xf, const bf16_t* wq, float* q) {
  __shared__ float y[B][D]; __shared__ float red[4]; __shared__ float part[8][B][32];
  for (int b = 0; b < B; ++b) ln_row(xf + b * D, y[b], red);
  int tid = threadIdx.x; int c = tid & 31, kg = tid >> 5;
  int n = blockIdx.x * 32 + c;
  float acc[B] = {0, 0, 0, 0};
  for (int k = kg * 80; k < kg * 80 + 80; ++k) { float w = bf2f(wq[(size_t)k * HD + n]); for (int b = 0; b < B; ++b) acc[b] += y[b][k] * w; }
  for (int b = 0; b < B; ++b) part[kg][b][c] = acc[b];
  __syncthreads();
  if (tid < 128) { int b = tid >> 5; float s = 0; for (int g = 0; g < 8; ++g) s += part[g][b][c]; q[b * HD + n] = s; }
}

__global__ void __launch_bounds__(256) k_attn(const float* q, const bf16_t* K, const bf16_t* V, float* pm, float* pl, float* po) {
  __shared__ float sm[4], sl[4], so[4][DH];
  int s = blockIdx.x, bh = blockIdx.y; int tid = threadIdx.x, lane = tid & 63, wave = tid >> 6;
  int r = lane >> 3, c = lane & 7;
  const float sc = 0.07216878364870322f * 1.4426950408889634f;
  unsigned qp[3][4];
  for (int t = 0; t < 3; ++t) for (int j = 0; j < 4; ++j) { int d = (c + 8 * t) * 8 + 2 * j; qp[t][j] = (unsigned)f2bf(q[bh * DH + d]) | ((unsigned)f2bf(q[bh * DH + d + 1]) << 16); }
  const int KPS = SK / NSPLIT;
  const bf16_t* kb = K + ((size_t)bh * SK + s * KPS) * DH;
  const bf16_t* vb = V + ((size_t)bh * SK + s * KPS) * DH;
  float m = -INFINITY, l = 0.f; float acc[24];
  for (int i = 0; i < 24; ++i) acc[i] = 0.f;
  for (int k0 = wave * 16; k0 < KPS; k0 += 64) {
    v4u kk[2][3], vv[2][3];
    for (int u = 0; u < 2; ++u) for (int t = 0; t < 3; ++t) {
      size_t off = (size_t)(k0 + u * 8 + r) * DH + (c + 8 * t) * 8;
      kk[u][t] = __builtin_nontemporal_load((const v4u*)(kb + off));
      vv[u][t] = __builtin_nontemporal_load((const v4u*)(vb + off));
    }
    float sco[2];
    for (int u = 0; u < 2; ++u) { float d = 0.f; for (int t = 0; t < 3; ++t) for (int j = 0; j < 4; ++j) d = dot2(kk[u][t][j], qp[t][j], d);
      d += __shfl_xor(d, 1); d += __shfl_xor(d, 2); d += __shfl_xor(d, 4); sco[u] = d * sc; }
    float mn = fmaxf(m, fmaxf(sco[0], sco[1]));
    float al = exp2f(m - mn); float p0 = exp2f(sco[0] - mn), p1 = exp2f(sco[1] - mn);
    l = l * al + p0 + p1; m = mn;
    for (int t = 0; t < 3; ++t) for (int j = 0; j < 4; ++j) {
      float a0 = __builtin_bit_cast(float, vv[0][t][j] << 16), a1 = __builtin_bit_cast(float, vv[0][t][j] & 0xffff0000u);
      float b0 = __builtin_bit_cast(float, vv[1][t][j] << 16), b1 = __builtin_bit_cast(float, vv[1][t][j] & 0xffff0000u);
      acc[t * 8 + 2 * j] = acc[t * 8 + 2 * j] * al + p0 * a0 + p1 * b0;
      acc[t * 8 + 2 * j + 1] = acc[t * 8 + 2 * j + 1] * al + p0 * a1 + p1 * b1;
    }
  }
  float M = m; for (int o = 8; o < 64; o <<= 1) M = fmaxf(M, __shfl_xor(M, o));
  float f = exp2f(m - M); l *= f;
  for (int o = 8; o < 64; o <<= 1) l += __shfl_xor(l, o);
  for (int i = 0; i < 24; ++i) { float a = acc[i] * f; for (int o = 8; o < 64; o <<= 1) a += __shfl_xor(a, o); acc[i] = a; }
  if (lane < 8) { for (int t = 0; t < 3; ++t) for (int j = 0; j < 8; ++j) so[wave][(c + 8 * t) * 8 + j] = acc[t * 8 + j]; if (lane == 0) { sm[wave] = M; sl[wave] = l; } }
  __syncthreads();
  if (tid < DH) {
    float MM = fmaxf(fmaxf(sm[0], sm[1]), fmaxf(sm[2], sm[3])); float L = 0, o = 0;
    for (int w = 0; w < 4; ++w) { float ff = exp2f(sm[w] - MM); L += sl[w] * ff; o += so[w][tid] * ff; }
    po[((size_t)bh * NSPLIT + s) * DH + tid] = o;
    if (tid == 0) { pm[bh * NSPLIT + s] = MM; pl[bh * NSPLIT + s] = L; }
  }
}

__global__ void __launch_bounds__(320) k_wo(const float* pm, const float* pl, const float* po, const bf16_t* wo, float* part) {
  __shared__ float o[B][48];
  int h = blockIdx.x >> 2, qd = blockIdx.x & 3; int tid = threadIdx.x;
  if (tid < B * 48) { int b = tid / 48, d = qd * 48 + tid % 48; int bh = b * H + h; float M = -INFINITY; for (int s = 0; s < NSPLIT; ++s) M = fmaxf(M, pm[bh * NSPLIT + s]);
    float L = 0, a = 0; for (int s = 0; s < NSPLIT; ++s) { float f = exp2f(pm[bh * NSPLIT + s] - M); L += f * pl[bh * NSPLIT + s]; a += f * po[((size_t)bh * NSPLIT + s) * DH + d]; }
    o[b][tid % 48] = a / L; }
  __syncthreads();
  float acc[B][2] = {};
  for (int d = 0; d < 48; ++d) { unsigned w = *(const unsigned*)(wo + (size_t)(h * DH + qd * 48 + d) * D + tid * 2);
    float w0 = __builtin_bit_cast(float, w << 16), w1 = __builtin_bit_cast(float, w & 0xffff0000u);
    for (int b = 0; b < B; ++b) { acc[b][0] += o[b][d] * w0; acc[b][1] += o[b][d] * w1; } }
  for (int b = 0; b < B; ++b) { part[((size_t)blockIdx.x * B + b) * D + tid * 2] = acc[b][0]; part[((size_t)blockIdx.x * B + b) * D + tid * 2 + 1] = acc[b][1]; }
}
__global__ void k_red(float* xf, const float* part, int np, bf16_t* out) {
  int i = blockIdx.x * blockDim.x + threadIdx.x; if (i >= B * D) return;
  float s = 0; for (int p = 0; p < np; ++p) s += part[(size_t)p * B * D + i];
  float v = xf[i] + s; xf[i] = v; if (out) out[i] = f2bf(v);
}
__global__ void __launch_bounds__(320) k_ffn(const float* xf, const bf16_t* wg, const bf16_t* wu, const bf16_t* wd, float* part) {
  __shared__ float y[B][D]; __shared__ float red[5]; __shared__ float pg[20][B][16], pu[20][B][16]; __shared__ float hh[B][16];
  for (int b = 0; b < B; ++b) ln_row(xf + b * D, y[b], red);
  int tid = threadIdx.x; int c = tid & 15, kg = tid >> 4;
  int f = blockIdx.x * 16 + c;
  float g[B] = {}, u[B] = {};
  for (int k = kg * 32; k < kg * 32 + 32; ++k) { float a = bf2f(wg[(size_t)k * DF + f]), bb = bf2f(wu[(size_t)k * DF + f]); for (int b = 0; b < B; ++b) { g[b] += y[b][k] * a; u[b] += y[b][k] * bb; } }
  for (int b = 0; b < B; ++b) { pg[kg][b][c] = g[b]; pu[kg][b][c] = u[b]; }
  __syncthreads();
  if (tid < 64) { int b = tid >> 4; float sg = 0, su = 0; for (int k = 0; k < 20; ++k) { sg += pg[k][b][c]; su += pu[k][b][c]; }
    float t = tanhf(0.7978845608028654f * (sg + 0.044715f * sg * sg * sg)); hh[b][c] = 0.5f * sg * (1.f + t) * su; }
  __syncthreads();
  float acc[B][2] = {};
  for (int j = 0; j < 16; ++j) { unsigned w = *(const unsigned*)(wd + (size_t)(blockIdx.x * 16 + j) * D + tid * 2);
    float w0 = __builtin_bit_cast(float, w << 16), w1 = __builtin_bit_cast(float, w & 0xffff0000u);
    for (int b = 0; b < B; ++b) { acc[b][0] += hh[b][j] * w0; acc[b][1] += hh[b][j] * w1; } }
  for (int b = 0; b < B; ++b) { part[((size_t)blockIdx.x * B + b) * D + tid * 2] = acc[b][0]; part[((size_t)blockIdx.x * B + b) * D + tid * 2 + 1] = acc[b][1]; }
}

extern "C" void kernel_launch(void* const* d_in, const int* in_sizes, int n_in, void* d_out, int out_size, void* d_ws, size_t ws_size, hipStream_t stream) {
  const bf16_t* x = (const bf16_t*)d_in[0]; const bf16_t* K = (const bf16_t*)d_in[1]; const bf16_t* V = (const bf16_t*)d_in[2];
  const bf16_t* wq = (const bf16_t*)d_in[3]; const bf16_t* wo = (const bf16_t*)d_in[4];
  const bf16_t* wg = (const bf16_t*)d_in[6]; const bf16_t* wu = (const bf16_t*)d_in[7]; const bf16_t* wd = (const bf16_t*)d_in[8];
  float* ws = (float*)d_ws;
  float* xf = ws; float* q = ws + 4096; float* pm = q + B * HD; float* pl = pm + 4096; float* po = pl + 4096; float* part = po + (size_t)B * H * NSPLIT * DH;
  k_init<<<10, 256, 0, stream>>>(x, xf);
  for (int layer = 0; layer < 2; ++layer) {
    k_q<<<HD / 32, 256, 0, stream>>>(xf, wq, q);
    k_attn<<<dim3(NSPLIT, B * H), 256, 0, stream>>>(q, K, V, pm, pl, po);
    k_wo<<<H * 4, 320, 0, stream>>>(pm, pl, po, wo, part);
    k_red<<<10, 256, 0, stream>>>(xf, part, H * 4, nullptr);
    k_ffn<<<DF / 16, 320, 0, stream>>>(xf, wg, wu, wd, part);
    k_red<<<10, 256, 0, stream>>>(xf, part, DF / 16, layer == 1 ? (bf16_t*)d_out : nullptr);
  }
}
```

```cpp
#include <hip/hip_runtime.h>
#include <stdint.h>

#define DI __device__ __forceinline__
#define LAS __attribute__((address_space(3)))
typedef unsigned v4u __attribute__((ext_vector_type(4)));
typedef unsigned v2u __attribute__((ext_vector_type(2)));
typedef float f32x2 __attribute__((ext_vector_type(2)));
typedef __bf16 bf16x2 __attribute__((ext_vector_type(2)));
typedef unsigned long long u64;

#define DBG_TS 0
constexpr int FLAGS_OFF = 4096;
constexpr int Q_OFF = 16384;
constexpr int X_OFF = 262144;
constexpr int P_OFF = 270336;
constexpr int PART_OFF = 1048576;
constexpr int DBG_OFF = 16777216;
constexpr int PART_CU = 1536;
constexpr int N_LASTQ = 4;
constexpr unsigned TAB_G = 0x20u;
constexpr unsigned TAB_B = 0x32132010u, TAB_L = 0xe4u, TAB_P = 0x2100100u, TAB_T = 0x51243121u;
constexpr int WS_BYTES = 20 << 20;
constexpr int LDS_BYTES = 163840;
constexpr int GU_OFF = 0, WQI_OFF = 122880, Y_OFF = 147456, Y_STR = 2688, Y_LO = 1344, OB_OFF = 158208, OB_LO = 448, RED_OFF = 159104, HB_OFF = 161152, RES_OFF = 162176, SQ_OFF = 162560, SG_OFF = 163328, YS_OFF = Y_OFF + 8192, OB2_OFF = WQI_OFF, RED2_OFF = WQI_OFF + 1024;
constexpr unsigned SPIN_CAP = 1u << 18;
constexpr int SC1 = 16;
#define TOUCH_WO 0
#define LATE_NOBAR 0
#define AG_SLEEP 1

template <int CTRL> DI float dppf(float x) { return __builtin_bit_cast(float, __builtin_amdgcn_mov_dpp(__builtin_bit_cast(int, x), CTRL, 0xf, 0xf, true)); }
DI float xrow_sum(float x) {
  auto s = __builtin_amdgcn_permlane16_swap(__float_as_uint(x), __float_as_uint(x), false, false);
  x = __uint_as_float(s[0]) + __uint_as_float(s[1]);
  auto t = __builtin_amdgcn_permlane32_swap(__float_as_uint(x), __float_as_uint(x), false, false);
  return __uint_as_float(t[0]) + __uint_as_float(t[1]);
}
DI float wave_sum(float x) {
  x += dppf<0xB1>(x); x += dppf<0x4E>(x); x += dppf<0x124>(x); x += dppf<0x128>(x);
  return xrow_sum(x);
}
DI unsigned pk_bf16(float lo, float hi) { f32x2 v = {lo, hi}; return __builtin_bit_cast(unsigned, __builtin_convertvector(v, bf16x2)); }
DI float bf_lo(unsigned p) { return __uint_as_float(p << 16); }
DI float bf_hi(unsigned p) { return __uint_as_float(p & 0xffff0000u); }
DI float rdl(float x, int l) { return __builtin_bit_cast(float, __builtin_amdgcn_readlane(__builtin_bit_cast(int, x), l)); }
DI void lds_w32(unsigned a, unsigned v) { *(LAS unsigned*)(uintptr_t)a = v; }
DI void lds_w16(unsigned a, unsigned short v) { *(LAS unsigned short*)(uintptr_t)a = v; }
DI float lds_rf(unsigned a) { return *(LAS float*)(uintptr_t)a; }
DI v4u mk_srd(const void* p, unsigned bytes) { u64 a = (u64)p; return v4u{(unsigned)a, (unsigned)(a >> 32) & 0xffffu, bytes, 0x00020000u}; }
DI int lane_id() { int l; asm volatile("v_mbcnt_lo_u32_b32 %0, -1, 0\n\tv_mbcnt_hi_u32_b32 %0, -1, %0" : "=v"(l)); return l; }
#define LANE() const int lane = lane_id(), c = lane >> 4, j = lane & 15; (void)c; (void)j
#define GIVE_UP() do { asm volatile("s_endpgm"); __builtin_unreachable(); } while (0)
#define LGKM0() asm volatile("s_waitcnt lgkmcnt(0)" ::: "memory")
#define VM0() asm volatile("s_waitcnt vmcnt(0)" ::: "memory")
#define BAR() asm volatile("s_waitcnt lgkmcnt(0)\n\ts_barrier" ::: "memory")
#define TCLOB "v16", "v17", "v18", "v19", "v20", "v21", "v22", "v23", "v24", "v25", "v26", "v27", "v28", "v29", "v30", "v31", "v32", "v33", "v34", "v35", "v36", "v37", "v38", "v39", "v40", "v41", "v42", "v43", "v44", "v45", "v46", "v47", "v48", "v49", "v50", "v51", "v52", "v53", "v54", "v55", "a0", "a1", "a2", "a3", "a4", "a5", "a6", "a7", "a8", "a9", "a10", "a11", "a12", "a13", "a14", "a15", "a16", "a17", "a18", "a19", "a20", "a21", "a22", "a23", "a24", "a25", "a26", "a27", "a28", "a29", "a30", "a31", "a32", "a33", "a34", "a35", "a36", "a37", "a38", "a39", "a40", "a41", "a42", "a43", "a44", "a45", "a46", "a47", "a48", "a49", "a50", "a51", "a52", "a53", "a54", "a55"
#define STAMP(k) do { } while (0)

DI void ln_to_lds(const float (&x)[10], unsigned ybase, int lane) {
  const float k0 = __builtin_bit_cast(float, __builtin_amdgcn_readfirstlane(__builtin_bit_cast(int, x[0])));
  float d[10], s = 0.f, v = 0.f;
#pragma unroll
  for (int i = 0; i < 10; ++i) { d[i] = x[i] - k0; s += d[i]; v = fmaf(d[i], d[i], v); }
  const float mean = wave_sum(s) * (1.0f / 640.0f);
  const float var = fmaxf(fmaf(-mean, mean, wave_sum(v) * (1.0f / 640.0f)), 0.f);
  const float rstd = __builtin_amdgcn_rsqf(var + 1e-5f);
#pragma unroll
  for (int i = 0; i < 5; ++i) {
    const float y0 = (d[2 * i] - mean) * rstd, y1 = (d[2 * i + 1] - mean) * rstd;
    const unsigned hi = pk_bf16(y0, y1), lo = pk_bf16(y0 - bf_lo(hi), y1 - bf_hi(hi));
    lds_w32(ybase + 256 * i + 4 * lane, hi); lds_w32(ybase + Y_LO + 256 * i + 4 * lane, lo);
  }
}

DI void gather_quarter(__amdgpu_buffer_rsrc_t rs, int src, unsigned tl, unsigned th, int w, unsigned a1, unsigned a2) {
  const int lane = lane_id(), e = lane < 40 ? lane : 39;
  v4u t;
  for (unsigned spins = 0;;) { asm volatile("" ::: "memory");
    t = __builtin_amdgcn_raw_buffer_load_b128(rs, src + w * 640 + e * 16, 0, SC1);
    if (__all(((t.x & 3u) == tl) & ((t.y & 3u) == th) & ((t.z & 3u) == tl) & ((t.w & 3u) == th))) break;
    if (++spins > SPIN_CAP) GIVE_UP();
    __builtin_amdgcn_s_sleep(AG_SLEEP);
  }
  const float x0 = __uint_as_float(t.x & ~3u), x1 = __uint_as_float(t.y & ~3u), x2 = __uint_as_float(t.z & ~3u), x3 = __uint_as_float(t.w & ~3u);
  const unsigned h0 = pk_bf16(x0, x1), h1 = pk_bf16(x2, x3);
  const unsigned l0 = pk_bf16(x0 - bf_lo(h0), x1 - bf_hi(h0)), l1 = pk_bf16(x2 - bf_lo(h1), x3 - bf_hi(h1));
  *(LAS v2u*)(uintptr_t)(YS_OFF + 320 * w + 8 * e) = v2u{h0, h1};
  *(LAS v2u*)(uintptr_t)(YS_OFF + 1280 + 320 * w + 8 * e) = v2u{l0, l1};
  const float m = lane < 40 ? 1.f : 0.f;
  const float s1 = wave_sum(m * ((x0 + x1) + (x2 + x3)));
  const float s2 = wave_sum(m * (fmaf(x0, x0, x1 * x1) + fmaf(x2, x2, x3 * x3)));
  if (lane == 0) { lds_w32(a1, __float_as_uint(s1)); lds_w32(a2, __float_as_uint(s2)); }
}

extern "C" __global__ void __launch_bounds__(256, 1) __attribute__((amdgpu_num_vgpr(56)))
fused_decode(const unsigned short* __restrict__ x0, const unsigned short* __restrict__ Kc, const unsigned short* __restrict__ Vc, const unsigned short* __restrict__ wq,
             const unsigned short* __restrict__ wo, const unsigned short* __restrict__ wg, const unsigned short* __restrict__ wu, const unsigned short* __restrict__ wd,
             unsigned short* out, unsigned char* ws) {
  const int bid = blockIdx.x, xg = bid & 7, jj = bid >> 3, h = xg * 4 + (jj >> 3), s = jj & 7;
  const int w = __builtin_amdgcn_readfirstlane(threadIdx.x >> 6);
  if (s < 7) {
    const unsigned off0 = ((unsigned)h * 1024u + (unsigned)w) * 3072u, nm = s;
    const unsigned flg = FLAGS_OFF + (unsigned)bid * 8u, hf = FLAGS_OFF + (unsigned)(((jj | 7) << 3) | xg) * 8u;
    unsigned char* part = ws + PART_OFF + (size_t)(h * 7 + s) * PART_CU;
    unsigned char* qp = ws + Q_OFF + (size_t)h * 4096;
    unsigned char* dbg = ws + DBG_OFF + (size_t)(bid * 4 + w) * 512;
    asm volatile(
    "s_mov_b64 s[36:37], %0\n\ts_mov_b64 s[40:41], %1\n\ts_mov_b64 s[64:65], %2\n\ts_mov_b32 s66, %3\n\ts_mov_b32 s67, %4\n\ts_mov_b32 s68, %5\n\ts_mov_b32 s69, %6\n\t"
    "s_mov_b64 s[70:71], %7\n\ts_mov_b64 s[72:73], %8\n\ts_mov_b32 s78, %9\n\ts_mov_b64 s[34:35], %10\n\t"
    "s_add_u32 s61, s67, 0\n\t"
    "s_sub_u32 s62, s61, 7\n\t"
    "s_cmp_ge_u32 s61, 7\n\t"
    "s_cselect_b32 s61, s62, s61\n\t"
    "s_cmp_lt_u32 s61, 4\n\t"
    "s_cselect_b32 s28, 13, 12\n\t"
    "s_mul_i32 s61, s61, 12288\n\t"
    "s_add_u32 s61, s61, s66\n\t"
    "s_add_u32 s24, s61, 0\n\t"
    "s_add_u32 s61, s67, 4\n\t"
    "s_sub_u32 s62, s61, 7\n\t"
    "s_cmp_ge_u32 s61, 7\n\t"
    "s_cselect_b32 s61, s62, s61\n\t"
    "s_cmp_lt_u32 s61, 4\n\t"
    "s_cselect_b32 s29, 13, 12\n\t"
    "s_mul_i32 s61, s61, 12288\n\t"
    "s_add_u32 s61, s61, s66\n\t"
    "s_add_u32 s25, s61, 100663296\n\t"
    "s_add_u32 s61, s67, 1\n\t"
    "s_sub_u32 s62, s61, 7\n\t"
    "s_cmp_ge_u32 s61, 7\n\t"
    "s_cselect_b32 s61, s62, s61\n\t"
    "s_cmp_lt_u32 s61, 4\n\t"
    "s_cselect_b32 s30, 13, 12\n\t"
    "s_mul_i32 s61, s61, 12288\n\t"
    "s_add_u32 s61, s61, s66\n\t"
    "s_add_u32 s26, s61, 201326592\n\t"
    "s_add_u32 s61, s67, 5\n\t"
    "s_sub_u32 s62, s61, 7\n\t"
    "s_cmp_ge_u32 s61, 7\n\t"
    "s_cselect_b32 s61, s62, s61\n\t"
    "s_cmp_lt_u32 s61, 4\n\t"
    "s_cselect_b32 s31, 13, 12\n\t"
    "s_mul_i32 s61, s61, 12288\n\t"
    "s_add_u32 s61, s61, s66\n\t"
    "s_add_u32 s27, s61, 301989888\n\t"
    "s_mul_i32 s61, s68, 36864\n\t"
    "s_sub_u32 s36, s36, s61\n\t"
    "s_subb_u32 s37, s37, 0\n\t"
    "s_and_b32 s37, s37, 0xffff\n\t"
    "s_add_u32 s38, s61, 402653184\n\t"
    "s_mov_b32 s39, 0x00020000\n\t"
    "s_and_b32 s41, s41, 0xffff\n\t"
    "s_mov_b32 s42, 402653184\n\t"
    "s_mov_b32 s43, 0x00020000\n\t"
    "s_mov_b32 s44, 0x03030303\n\t"
    "s_mov_b32 s45, 0x03030303\n\t"
    "s_mov_b32 s46, 0x0c0c0c0c\n\t"
    "s_mov_b32 s47, 0x0c0c0c0c\n\t"
    "s_mov_b32 s48, 0x30303030\n\t"
    "s_mov_b32 s49, 0x30303030\n\t"
    "s_mov_b32 s50, 0xc0c0c0c0\n\t"
    "s_mov_b32 s51, 0xc0c0c0c0\n\t"
    "s_mov_b32 s52, 0x55555555\n\t"
    "s_mov_b32 s53, 0x55555555\n\t"
    "s_mov_b32 s54, 0x0F0F0F0F\n\t"
    "s_mov_b32 s55, 0xF0F0F0F0\n\t"
    "s_mov_b32 s32, 0x0F0F0F0F\n\t"
    "s_mov_b32 s33, 0x0F0F0F0F\n\t"
    "v_mov_b32 v40, 0xffff0000\n\t"
    "v_mov_b32 v41, 0xffff\n\t"
    "v_cndmask_b32 v40, v40, v41, s[52:53]\n\t"
    "v_cndmask_b32 v36, 0, v40, s[44:45]\n\t"
    "v_cndmask_b32 v37, 0, v40, s[46:47]\n\t"
    "v_cndmask_b32 v38, 0, v40, s[48:49]\n\t"
    "v_cndmask_b32 v39, 0, v40, s[50:51]\n\t"
    "v_mbcnt_lo_u32_b32 v0, -1, 0\n\t"
    "v_mbcnt_hi_u32_b32 v0, -1, v0\n\t"
    "v_lshrrev_b32 v1, 4, v0\n\t"
    "v_and_b32 v2, 15, v0\n\t"
    "v_lshrrev_b32 v3, 2, v2\n\t"
    "v_mul_u32_u24 v29, 768, v3\n\t"
    "v_lshl_add_u32 v29, v1, 6, v29\n\t"
    "v_and_b32 v3, 3, v2\n\t"
    "v_lshl_add_u32 v29, v3, 4, v29\n\t"
    "v_add_u32 v29, s61, v29\n\t"
    "v_mul_u32_u24 v30, 384, v1\n\t"
    "v_lshl_add_u32 v30, v2, 4, v30\n\t"
    "v_mov_b32 v3, 1408\n\t"
    "v_mov_b32 v4, 256\n\t"
    "v_cmp_gt_u32 vcc, 8, v2\n\t"
    "s_nop 1\n\t"
    "v_cndmask_b32 v3, v3, v4, vcc\n\t"
    "v_add_u32 v31, v30, v3\n\t"
    "s_mov_b32 s76, 1\n\t"
    "s_mov_b32 s77, 150528\n\t"
    "s_cmp_lg_u32 s68, 0\n\t"
    "s_cbranch_scc1 .Lat_ncl_1\n\t"
    "v_mbcnt_lo_u32_b32 v0, -1, 0\n\t"
    "v_mbcnt_hi_u32_b32 v0, -1, v0\n\t"
    "v_lshlrev_b32 v0, 3, v0\n\t"
    "v_mov_b32 v4, 0\n\t"
    "v_mov_b32 v5, 0\n\t"
    "global_store_dwordx2 v0, v[4:5], s[70:71] offset:0 sc1\n\t"
    "global_store_dwordx2 v0, v[4:5], s[70:71] offset:512 sc1\n\t"
    "global_store_dwordx2 v0, v[4:5], s[70:71] offset:1024 sc1\n\t"
    ".Lat_ncl_1:\n\t"
    "s_add_u32 s92, s24, 0\n\t"
    "buffer_load_dwordx4 v[80:83], v29, s[36:39], s92 offen offset:0 nt\n\t"
    "buffer_load_dwordx4 v[84:87], v29, s[36:39], s92 offen offset:256 nt\n\t"
    "buffer_load_dwordx4 v[88:91], v29, s[36:39], s92 offen offset:512 nt\n\t"
    "buffer_load_dwordx4 v[92:95], v30, s[40:43], s92 offen nt\n\t"
    "buffer_load_dwordx4 v[96:99], v31, s[40:43], s92 offen nt\n\t"
    "buffer_load_dwordx4 v[100:103], v30, s[40:43], s92 offen offset:1664 nt\n\t"
    "s_add_u32 s92, s24, 86016\n\t"
    "buffer_load_dwordx4 v[104:107], v29, s[36:39], s92 offen offset:0 nt\n\t"
    "buffer_load_dwordx4 v[108:111], v29, s[36:39], s92 offen offset:256 nt\n\t"
    "buffer_load_dwordx4 v[112:115], v29, s[36:39], s92 offen offset:512 nt\n\t"
    "buffer_load_dwordx4 v[116:119], v30, s[40:43], s92 offen nt\n\t"
    "buffer_load_dwordx4 v[120:123], v31, s[40:43], s92 offen nt\n\t"
    "buffer_load_dwordx4 v[124:127], v30, s[40:43], s92 offen offset:1664 nt\n\t"
    "v_mov_b32 v8, 0\n\t"
    "global_load_dwordx2 v[6:7], v8, s[64:65] sc1\n\t"
    "s_add_u32 s92, s24, 172032\n\t"
    "s_waitcnt vmcnt(9)\n\t"
    "buffer_load_dwordx4 v[128:131], v29, s[36:39], s92 offen offset:0 nt\n\t"
    "buffer_load_dwordx4 v[132:135], v29, s[36:39], s92 offen offset:256 nt\n\t"
    "buffer_load_dwordx4 v[136:139], v29, s[36:39], s92 offen offset:512 nt\n\t"
    "s_waitcnt vmcnt(9)\n\t"
    "buffer_load_dwordx4 v[140:143], v30, s[40:43], s92 offen nt\n\t"
    "buffer_load_dwordx4 v[144:147], v31, s[40:43], s92 offen nt\n\t"
    "buffer_load_dwordx4 v[148:151], v30, s[40:43], s92 offen offset:1664 nt\n\t"
    "s_add_u32 s92, s24, 258048\n\t"
    "s_waitcnt vmcnt(9)\n\t"
    "buffer_load_dwordx4 v[152:155], v29, s[36:39], s92 offen offset:0 nt\n\t"
    "buffer_load_dwordx4 v[156:159], v29, s[36:39], s92 offen offset:256 nt\n\t"
    "buffer_load_dwordx4 v[160:163], v29, s[36:39], s92 offen offset:512 nt\n\t"
    "s_waitcnt vmcnt(9)\n\t"
    "buffer_load_dwordx4 v[164:167], v30, s[40:43], s92 offen nt\n\t"
    "buffer_load_dwordx4 v[168:171], v31, s[40:43], s92 offen nt\n\t"
    "buffer_load_dwordx4 v[172:175], v30, s[40:43], s92 offen offset:1664 nt\n\t"
    "s_add_u32 s92, s24, 344064\n\t"
    "s_waitcnt vmcnt(9)\n\t"
    "buffer_load_dwordx4 v[176:179], v29, s[36:39], s92 offen offset:0 nt\n\t"
    "buffer_load_dwordx4 v[180:183], v29, s[36:39], s92 offen offset:256 nt\n\t"
    "buffer_load_dwordx4 v[184:187], v29, s[36:39], s92 offen offset:512 nt\n\t"
    "s_waitcnt vmcnt(9)\n\t"
    "buffer_load_dwordx4 v[188:191], v30, s[40:43], s92 offen nt\n\t"
    "buffer_load_dwordx4 v[192:195], v31, s[40:43], s92 offen nt\n\t"
    "buffer_load_dwordx4 v[196:199], v30, s[40:43], s92 offen offset:1664 nt\n\t"
    "s_add_u32 s92, s24, 430080\n\t"
    "s_waitcnt vmcnt(9)\n\t"
    "buffer_load_dwordx4 v[200:203], v29, s[36:39], s92 offen offset:0 nt\n\t"
    "buffer_load_dwordx4 v[204:207], v29, s[36:39], s92 offen offset:256 nt\n\t"
    "buffer_load_dwordx4 v[208:211], v29, s[36:39], s92 offen offset:512 nt\n\t"
    "s_waitcnt vmcnt(9)\n\t"
    "buffer_load_dwordx4 v[212:215], v30, s[40:43], s92 offen nt\n\t"
    "buffer_load_dwordx4 v[216:219], v31, s[40:43], s92 offen nt\n\t"
    "buffer_load_dwordx4 v[220:223], v30, s[40:43], s92 offen offset:1664 nt\n\t"
    "s_add_u32 s92, s24, 516096\n\t"
    "s_waitcnt vmcnt(9)\n\t"
    "buffer_load_dwordx4 v[224:227], v29, s[36:39], s92 offen offset:0 nt\n\t"
    "buffer_load_dwordx4 v[228:231], v29, s[36:39], s92 offen offset:256 nt\n\t"
    "buffer_load_dwordx4 v[232:235], v29, s[36:39], s92 offen offset:512 nt\n\t"
    "s_waitcnt vmcnt(9)\n\t"
    "buffer_load_dwordx4 v[236:239], v30, s[40:43], s92 offen nt\n\t"
    "buffer_load_dwordx4 v[240:243], v31, s[40:43], s92 offen nt\n\t"
    "buffer_load_dwordx4 v[244:247], v30, s[40:43], s92 offen offset:1664 nt\n\t"
    "s_add_u32 s92, s24, 602112\n\t"
    "s_waitcnt vmcnt(9)\n\t"
    "buffer_load_dwordx4 v[248:251], v29, s[36:39], s92 offen offset:0 nt\n\t"
    "buffer_load_dwordx4 v[252:255], v29, s[36:39], s92 offen offset:256 nt\n\t"
    "buffer_load_dwordx4 a[0:3], v29, s[36:39], s92 offen offset:512 nt\n\t"
    "s_waitcnt vmcnt(9)\n\t"
    "buffer_load_dwordx4 a[4:7], v30, s[40:43], s92 offen nt\n\t"
    "buffer_load_dwordx4 a[8:11], v31, s[40:43], s92 offen nt\n\t"
    "buffer_load_dwordx4 a[12:15], v30, s[40:43], s92 offen offset:1664 nt\n\t"
    "s_add_u32 s92, s24, 688128\n\t"
    "s_waitcnt vmcnt(9)\n\t"
    "buffer_load_dwordx4 a[16:19], v29, s[36:39], s92 offen offset:0 nt\n\t"
    "buffer_load_dwordx4 a[20:23], v29, s[36:39], s92 offen offset:256 nt\n\t"
    "buffer_load_dwordx4 a[24:27], v29, s[36:39], s92 offen offset:512 nt\n\t"
    "s_waitcnt vmcnt(9)\n\t"
    "buffer_load_dwordx4 a[28:31], v30, s[40:43], s92 offen nt\n\t"
    "buffer_load_dwordx4 a[32:35], v31, s[40:43], s92 offen nt\n\t"
    "buffer_load_dwordx4 a[36:39], v30, s[40:43], s92 offen offset:1664 nt\n\t"
    "s_add_u32 s92, s24, 774144\n\t"
    "s_waitcnt vmcnt(9)\n\t"
    "buffer_load_dwordx4 a[40:43], v29, s[36:39], s92 offen offset:0 nt\n\t"
    "buffer_load_dwordx4 a[44:47], v29, s[36:39], s92 offen offset:256 nt\n\t"
    "buffer_load_dwordx4 a[48:51], v29, s[36:39], s92 offen offset:512 nt\n\t"
    "s_waitcnt vmcnt(9)\n\t"
    "buffer_load_dwordx4 a[52:55], v30, s[40:43], s92 offen nt\n\t"
    "buffer_load_dwordx4 a[56:59], v31, s[40:43], s92 offen nt\n\t"
    "buffer_load_dwordx4 a[60:63], v30, s[40:43], s92 offen offset:1664 nt\n\t"
    "s_add_u32 s92, s24, 860160\n\t"
    "s_waitcnt vmcnt(9)\n\t"
    "buffer_load_dwordx4 a[64:67], v29, s[36:39], s92 offen offset:0 nt\n\t"
    "buffer_load_dwordx4 a[68:71], v29, s[36:39], s92 offen offset:256 nt\n\t"
    "buffer_load_dwordx4 a[72:75], v29, s[36:39], s92 offen offset:512 nt\n\t"
    "s_waitcnt vmcnt(9)\n\t"
    "buffer_load_dwordx4 a[76:79], v30, s[40:43], s92 offen nt\n\t"
    "buffer_load_dwordx4 a[80:83], v31, s[40:43], s92 offen nt\n\t"
    "buffer_load_dwordx4 a[84:87], v30, s[40:43], s92 offen offset:1664 nt\n\t"
    "v_readfirstlane_b32 s74, v6\n\t"
    "v_readfirstlane_b32 s75, v7\n\t"
    "s_add_u32 s74, s74, 1\n\t"
    "s_addc_u32 s75, s75, 0\n\t"
    "v_mov_b32 v0, s78\n\t"
    "s_mov_b32 s98, 0\n\t"
    "global_load_dwordx2 v[2:3], v0, s[64:65] sc1\n\t"
    "s_add_u32 s92, s24, 946176\n\t"
    "s_waitcnt vmcnt(9)\n\t"
    "buffer_load_dwordx4 a[88:91], v29, s[36:39], s92 offen offset:0 nt\n\t"
    "buffer_load_dwordx4 a[92:95], v29, s[36:39], s92 offen offset:256 nt\n\t"
    "buffer_load_dwordx4 a[96:99], v29, s[36:39], s92 offen offset:512 nt\n\t"
    "s_waitcnt vmcnt(9)\n\t"
    "buffer_load_dwordx4 a[100:103], v30, s[40:43], s92 offen nt\n\t"
    "buffer_load_dwordx4 a[104:107], v31, s[40:43], s92 offen nt\n\t"
    "buffer_load_dwordx4 a[108:111], v30, s[40:43], s92 offen offset:1664 nt\n\t"
    "s_waitcnt vmcnt(6)\n\t"
    "v_cmp_eq_u64 vcc, s[74:75], v[2:3]\n\t"
    "s_cmp_eq_u64 vcc, -1\n\t"
    "s_cbranch_scc1 .Lat_fl_3\n\t"
    ".Lat_fl_2:\n\t"
    "global_load_dwordx2 v[2:3], v0, s[64:65] sc1\n\t"
    "s_waitcnt vmcnt(0)\n\t"
    "v_cmp_eq_u64 vcc, s[74:75], v[2:3]\n\t"
    "s_cmp_eq_u64 vcc, -1\n\t"
    "s_cbranch_scc1 .Lat_fl_3\n\t"
    "s_sleep 2\n\t"
    "s_add_u32 s98, s98, 1\n\t"
    "s_cmp_lt_u32 s98, 0x40000\n\t"
    "s_cbranch_scc1 .Lat_fl_2\n\t"
    "s_endpgm\n\t"
    ".Lat_fl_3:\n\t"
    "s_cmp_lg_u32 s68, 0\n\t"
    "s_cbranch_scc1 .Lat_nfl_4\n\t"
    "v_mov_b32 v0, s69\n\t"
    "v_mov_b32 v2, s74\n\t"
    "v_mov_b32 v3, s75\n\t"
    "s_mov_b64 exec, 1\n\t"
    "global_store_dwordx2 v0, v[2:3], s[64:65] sc1\n\t"
    "s_mov_b64 exec, -1\n\t"
    ".Lat_nfl_4:\n\t"
    "s_add_u32 s92, s24, 1032192\n\t"
    "s_waitcnt vmcnt(9)\n\t"
    "buffer_load_dwordx4 a[112:115], v29, s[36:39], s92 offen offset:0 nt\n\t"
    "buffer_load_dwordx4 a[116:119], v29, s[36:39], s92 offen offset:256 nt\n\t"
    "buffer_load_dwordx4 a[120:123], v29, s[36:39], s92 offen offset:512 nt\n\t"
    "s_waitcnt vmcnt(9)\n\t"
    "buffer_load_dwordx4 a[124:127], v30, s[40:43], s92 offen nt\n\t"
    "buffer_load_dwordx4 a[128:131], v31, s[40:43], s92 offen nt\n\t"
    "buffer_load_dwordx4 a[132:135], v30, s[40:43], s92 offen offset:1664 nt\n\t"
    "s_add_u32 s92, s24, 1118208\n\t"
    "s_waitcnt vmcnt(9)\n\t"
    "buffer_load_dwordx4 a[136:139], v29, s[36:39], s92 offen offset:0 nt\n\t"
    "buffer_load_dwordx4 a[140:143], v29, s[36:39], s92 offen offset:256 nt\n\t"
    "buffer_load_dwordx4 a[144:147], v29, s[36:39], s92 offen offset:512 nt\n\t"
    "s_waitcnt vmcnt(9)\n\t"
    "buffer_load_dwordx4 a[148:151], v30, s[40:43], s92 offen nt\n\t"
    "buffer_load_dwordx4 a[152:155], v31, s[40:43], s92 offen nt\n\t"
    "buffer_load_dwordx4 a[156:159], v30, s[40:43], s92 offen offset:1664 nt\n\t"
    "s_add_u32 s92, s24, 1204224\n\t"
    "s_waitcnt vmcnt(9)\n\t"
    "buffer_load_dwordx4 a[160:163], v29, s[36:39], s92 offen offset:0 nt\n\t"
    "buffer_load_dwordx4 a[164:167], v29, s[36:39], s92 offen offset:256 nt\n\t"
    "buffer_load_dwordx4 a[168:171], v29, s[36:39], s92 offen offset:512 nt\n\t"
    "s_waitcnt vmcnt(9)\n\t"
    "buffer_load_dwordx4 a[172:175], v30, s[40:43], s92 offen nt\n\t"
    "buffer_load_dwordx4 a[176:179], v31, s[40:43], s92 offen nt\n\t"
    "buffer_load_dwordx4 a[180:183], v30, s[40:43], s92 offen offset:1664 nt\n\t"
    "v_mbcnt_lo_u32_b32 v0, -1, 0\n\t"
    "v_mbcnt_hi_u32_b32 v0, -1, v0\n\t"
    "v_lshrrev_b32 v1, 4, v0\n\t"
    "v_mul_u32_u24 v1, 96, v1\n\t"
    "v_and_b32 v2, 15, v0\n\t"
    "v_lshl_add_u32 v1, v2, 1, v1\n\t"
    "v_lshlrev_b32 v0, 3, v0\n\t"
    "v_add_u32 v1, 147456, v1\n\t"
    "s_mov_b32 s98, 0\n\t"
    "global_load_dwordx2 v[16:17], v0, s[72:73] offset:0 sc1\n\t"
    "global_load_dwordx2 v[18:19], v0, s[72:73] offset:512 sc1\n\t"
    "global_load_dwordx2 v[20:21], v0, s[72:73] offset:1024 sc1\n\t"
    "global_load_dwordx2 v[22:23], v0, s[72:73] offset:1536 sc1\n\t"
    "s_add_u32 s92, s24, 1290240\n\t"
    "s_waitcnt vmcnt(9)\n\t"
    "buffer_load_dwordx4 a[184:187], v29, s[36:39], s92 offen offset:0 nt\n\t"
    "buffer_load_dwordx4 a[188:191], v29, s[36:39], s92 offen offset:256 nt\n\t"
    "buffer_load_dwordx4 a[192:195], v29, s[36:39], s92 offen offset:512 nt\n\t"
    "s_waitcnt vmcnt(9)\n\t"
    "buffer_load_dwordx4 a[196:199], v30, s[40:43], s92 offen nt\n\t"
    "buffer_load_dwordx4 a[200:203], v31, s[40:43], s92 offen nt\n\t"
    "buffer_load_dwordx4 a[204:207], v30, s[40:43], s92 offen offset:1664 nt\n\t"
    "s_waitcnt vmcnt(6)\n\t"
    "s_branch .Lat_q1_7\n\t"
    ".Lat_q1_5:\n\t"
    "global_load_dwordx2 v[16:17], v0, s[72:73] offset:0 sc1\n\t"
    "global_load_dwordx2 v[18:19], v0, s[72:73] offset:512 sc1\n\t"
    "global_load_dwordx2 v[20:21], v0, s[72:73] offset:1024 sc1\n\t"
    "global_load_dwordx2 v[22:23], v0, s[72:73] offset:1536 sc1\n\t"
    "s_waitcnt vmcnt(0)\n\t"
    ".Lat_q1_7:\n\t"
    "v_and_b32 v2, v17, v19\n\t"
    "v_and_b32 v3, v21, v23\n\t"
    "v_and_b32 v2, v2, v3\n\t"
    "v_lshrrev_b32 v2, 16, v2\n\t"
    "v_cmp_eq_u32 vcc, 1, v2\n\t"
    "s_cmp_eq_u64 vcc, -1\n\t"
    "s_cbranch_scc1 .Lat_q1_6\n\t"
    "s_sleep 4\n\t"
    "s_add_u32 s98, s98, 1\n\t"
    "s_cmp_lt_u32 s98, 0x40000\n\t"
    "s_cbranch_scc1 .Lat_q1_5\n\t"
    "s_endpgm\n\t"
    ".Lat_q1_6:\n\t"
    "ds_write_b16 v1, v16 offset:0\n\t"
    "ds_write_b16_d16_hi v1, v16 offset:32\n\t"
    "ds_write_b16 v1, v17 offset:64\n\t"
    "ds_write_b16 v1, v18 offset:384\n\t"
    "ds_write_b16_d16_hi v1, v18 offset:416\n\t"
    "ds_write_b16 v1, v19 offset:448\n\t"
    "ds_write_b16 v1, v20 offset:768\n\t"
    "ds_write_b16_d16_hi v1, v20 offset:800\n\t"
    "ds_write_b16 v1, v21 offset:832\n\t"
    "ds_write_b16 v1, v22 offset:1152\n\t"
    "ds_write_b16_d16_hi v1, v22 offset:1184\n\t"
    "ds_write_b16 v1, v23 offset:1216\n\t"
    "s_waitcnt lgkmcnt(0)\n\t"
    "s_mov_b32 s97, 147456\n\t"
    "s_call_b64 s[80:81], .Lat_init\n\t"
    "s_add_u32 s83, s24, 0\n\t"
    "s_mov_b32 s93, 16\n\t"
    "s_mov_b32 s94, 20\n\t"
    "s_mov_b32 s95, 18\n\t"
    "s_add_u32 s84, s24, 1548288\n\t"
    "s_add_u32 s85, s24, 1634304\n\t"
    "s_call_b64 s[80:81], .Lat_a1\n\t"
    "s_add_i32 s82, s28, 6\n\t"
    "s_add_u32 s83, s24, 1720320\n\t"
    "s_add_u32 s84, s25, 2064384\n\t"
    "s_add_u32 s85, s25, 2150400\n\t"
    "s_mov_b32 s86, 6\n\t"
    "s_mov_b32 s89, 6\n\t"
    "s_mov_b32 s90, 6\n\t"
    "s_mov_b32 s57, 0\n\t"
    "s_call_b64 s[80:81], .Lat_wl\n\t"
    "s_mov_b32 s79, 0x10000003\n\t"
    "s_mov_b32 s99, 156928\n\t"
    "s_mov_b32 s56, 0\n\t"
    "s_call_b64 s[80:81], .Lat_merge\n\t"
    "s_mov_b32 s97, 147840\n\t"
    "s_call_b64 s[80:81], .Lat_init\n\t"
    "s_add_i32 s82, s29, 0\n\t"
    "s_add_u32 s83, s25, 2236416\n\t"
    "s_add_u32 s84, s26, 2064384\n\t"
    "s_add_u32 s85, s26, 2150400\n\t"
    "s_mov_b32 s86, 0\n\t"
    "s_mov_b32 s89, 0\n\t"
    "s_mov_b32 s90, 0\n\t"
    "s_mov_b32 s57, 4\n\t"
    "s_call_b64 s[80:81], .Lat_wl\n\t"
    "s_mov_b32 s79, 0x1\n\t"
    "s_mov_b32 s99, 156928\n\t"
    "s_mov_b32 s56, 0\n\t"
    "s_call_b64 s[80:81], .Lat_merge\n\t"
    "s_mov_b32 s97, 148224\n\t"
    "s_call_b64 s[80:81], .Lat_init\n\t"
    "s_mov_b32 s82, 5\n\t"
    "s_add_u32 s83, s26, 2236416\n\t"
    "s_add_u32 s84, s24, 2064384\n\t"
    "s_add_u32 s85, s24, 2150400\n\t"
    "s_cmp_eq_u32 s68, 0\n\t"
    "s_cselect_b32 s84, 402653184, s84\n\t"
    "s_cselect_b32 s85, 402653184, s85\n\t"
    "s_mov_b32 s86, 0\n\t"
    "s_mov_b32 s89, 0\n\t"
    "s_mov_b32 s90, 0\n\t"
    "s_mov_b32 s57, 0\n\t"
    "s_call_b64 s[80:81], .Lat_wl\n\t"
    "s_mov_b32 s97, 2048\n\t"
    "s_call_b64 s[80:81], .Lat_prepoll\n\t"
    "s_mov_b32 s79, 0x1\n\t"
    "s_mov_b32 s99, 157728\n\t"
    "s_mov_b32 s56, 0\n\t"
    "s_call_b64 s[80:81], .Lat_merge\n\t"
    "s_add_u32 s84, s24, 2064384\n\t"
    "s_add_u32 s85, s24, 2150400\n\t"
    "s_mov_b32 s97, 2048\n\t"
    "s_mov_b32 s61, 148992\n\t"
    "s_call_b64 s[80:81], .Lat_pollq\n\t"
    "s_mov_b32 s97, 148992\n\t"
    "s_call_b64 s[80:81], .Lat_init\n\t"
    "s_add_u32 s83, s25, 0\n\t"
    "s_add_u32 s96, s24, 2064384\n\t"
    "s_mov_b32 s94, 20\n\t"
    "s_call_b64 s[80:81], .Lat_a2\n\t"
    "s_mov_b32 s79, 0x1\n\t"
    "s_mov_b32 s99, 158528\n\t"
    "s_mov_b32 s56, 0\n\t"
    "s_call_b64 s[80:81], .Lat_merge\n\t"
    "s_mov_b32 s97, 147840\n\t"
    "s_call_b64 s[80:81], .Lat_init\n\t"
    "s_add_u32 s83, s25, 0\n\t"
    "s_mov_b32 s93, 9\n\t"
    "s_mov_b32 s94, 20\n\t"
    "s_mov_b32 s95, 18\n\t"
    "s_add_u32 s84, s25, 1548288\n\t"
    "s_add_u32 s85, s25, 1634304\n\t"
    "s_call_b64 s[80:81], .Lat_a1\n\t"
    "s_mov_b32 s82, 6\n\t"
    "s_add_u32 s83, s25, 1720320\n\t"
    "s_add_u32 s84, s26, 2494464\n\t"
    "s_add_u32 s85, s26, 2580480\n\t"
    "s_mov_b32 s86, 6\n\t"
    "s_mov_b32 s89, 6\n\t"
    "s_mov_b32 s90, 6\n\t"
    "s_mov_b32 s57, 0\n\t"
    "s_call_b64 s[80:81], .Lat_wl\n\t"
    "s_mov_b32 s79, 0x20000002\n\t"
    "s_mov_b32 s99, 156928\n\t"
    "s_mov_b32 s56, 0\n\t"
    "s_call_b64 s[80:81], .Lat_merge\n\t"
    "s_mov_b32 s97, 148224\n\t"
    "s_call_b64 s[80:81], .Lat_init\n\t"
    "s_add_i32 s82, s30, -5\n\t"
    "s_add_u32 s83, s26, 2666496\n\t"
    "s_add_u32 s84, s24, 2236416\n\t"
    "s_add_u32 s85, s24, 2322432\n\t"
    "s_mov_b32 s86, 0\n\t"
    "s_mov_b32 s89, 0\n\t"
    "s_mov_b32 s90, 0\n\t"
    "s_mov_b32 s57, 4\n\t"
    "s_call_b64 s[80:81], .Lat_wl\n\t"
    "s_mov_b32 s79, 0x0\n\t"
    "s_mov_b32 s99, 157728\n\t"
    "s_mov_b32 s56, 0\n\t"
    "s_call_b64 s[80:81], .Lat_merge\n\t"
    "s_mov_b32 s97, 148992\n\t"
    "s_call_b64 s[80:81], .Lat_init\n\t"
    "s_add_i32 s82, s28, -2\n\t"
    "s_add_u32 s83, s24, 2408448\n\t"
    "s_add_u32 s84, s25, 2064384\n\t"
    "s_add_u32 s85, s25, 2150400\n\t"
    "s_cmp_eq_u32 s68, 0\n\t"
    "s_cselect_b32 s84, 402653184, s84\n\t"
    "s_cselect_b32 s85, 402653184, s85\n\t"
    "s_mov_b32 s86, 0\n\t"
    "s_mov_b32 s89, 0\n\t"
    "s_mov_b32 s90, 0\n\t"
    "s_mov_b32 s57, 0\n\t"
    "s_call_b64 s[80:81], .Lat_wl\n\t"
    "s_mov_b32 s97, 2560\n\t"
    "s_call_b64 s[80:81], .Lat_prepoll\n\t"
    "s_mov_b32 s79, 0x10000002\n\t"
    "s_mov_b32 s99, 158528\n\t"
    "s_mov_b32 s56, 512\n\t"
    "s_call_b64 s[80:81], .Lat_merge\n\t"
    "s_add_u32 s84, s25, 2064384\n\t"
    "s_add_u32 s85, s25, 2150400\n\t"
    "s_mov_b32 s97, 2560\n\t"
    "s_mov_b32 s61, 149376\n\t"
    "s_call_b64 s[80:81], .Lat_pollq\n\t"
    "s_mov_b32 s97, 149376\n\t"
    "s_call_b64 s[80:81], .Lat_init\n\t"
    "s_add_u32 s83, s26, 0\n\t"
    "s_add_u32 s96, s25, 2064384\n\t"
    "s_mov_b32 s94, 20\n\t"
    "s_call_b64 s[80:81], .Lat_a2\n\t"
    "s_mov_b32 s79, 0x1\n\t"
    "s_mov_b32 s99, 159328\n\t"
    "s_mov_b32 s56, 0\n\t"
    "s_call_b64 s[80:81], .Lat_merge\n\t"
    "s_mov_b32 s97, 148224\n\t"
    "s_call_b64 s[80:81], .Lat_init\n\t"
    "s_add_u32 s83, s26, 0\n\t"
    "s_mov_b32 s93, 9\n\t"
    "s_mov_b32 s94, 20\n\t"
    "s_mov_b32 s95, 18\n\t"
    "s_add_u32 s84, s26, 1548288\n\t"
    "s_add_u32 s85, s26, 1634304\n\t"
    "s_call_b64 s[80:81], .Lat_a1\n\t"
    "s_mov_b32 s82, 6\n\t"
    "s_add_u32 s83, s26, 1720320\n\t"
    "s_add_u32 s84, s27, 2064384\n\t"
    "s_add_u32 s85, s27, 2150400\n\t"
    "s_mov_b32 s86, 6\n\t"
    "s_mov_b32 s89, 6\n\t"
    "s_mov_b32 s90, 6\n\t"
    "s_mov_b32 s57, 0\n\t"
    "s_call_b64 s[80:81], .Lat_wl\n\t"
    "s_mov_b32 s79, 0x30000002\n\t"
    "s_mov_b32 s99, 157728\n\t"
    "s_mov_b32 s56, 0\n\t"
    "s_call_b64 s[80:81], .Lat_merge\n\t"
    "s_mov_b32 s97, 148608\n\t"
    "s_call_b64 s[80:81], .Lat_init\n\t"
    "s_add_i32 s82, s31, 0\n\t"
    "s_add_u32 s83, s27, 2236416\n\t"
    "s_add_u32 s84, s25, 2236416\n\t"
    "s_add_u32 s85, s25, 2322432\n\t"
    "s_mov_b32 s86, 0\n\t"
    "s_mov_b32 s89, 0\n\t"
    "s_mov_b32 s90, 0\n\t"
    "s_mov_b32 s57, 4\n\t"
    "s_call_b64 s[80:81], .Lat_wl\n\t"
    "s_mov_b32 s79, 0x1\n\t"
    "s_mov_b32 s99, 160128\n\t"
    "s_mov_b32 s56, 0\n\t"
    "s_call_b64 s[80:81], .Lat_merge\n\t"
    "s_mov_b32 s97, 149376\n\t"
    "s_call_b64 s[80:81], .Lat_init\n\t"
    "s_mov_b32 s82, 5\n\t"
    "s_add_u32 s83, s25, 2408448\n\t"
    "s_add_u32 s84, s26, 2064384\n\t"
    "s_add_u32 s85, s26, 2150400\n\t"
    "s_cmp_eq_u32 s68, 0\n\t"
    "s_cselect_b32 s84, 402653184, s84\n\t"
    "s_cselect_b32 s85, 402653184, s85\n\t"
    "s_mov_b32 s86, 0\n\t"
    "s_mov_b32 s89, 0\n\t"
    "s_mov_b32 s90, 0\n\t"
    "s_mov_b32 s57, 0\n\t"
    "s_call_b64 s[80:81], .Lat_wl\n\t"
    "s_mov_b32 s97, 3072\n\t"
    "s_call_b64 s[80:81], .Lat_prepoll\n\t"
    "s_mov_b32 s79, 0x0\n\t"
    "s_mov_b32 s99, 159328\n\t"
    "s_mov_b32 s56, 0\n\t"
    "s_call_b64 s[80:81], .Lat_merge\n\t"
    "s_add_u32 s84, s26, 2064384\n\t"
    "s_add_u32 s85, s26, 2150400\n\t"
    "s_mov_b32 s97, 3072\n\t"
    "s_mov_b32 s61, 149760\n\t"
    "s_call_b64 s[80:81], .Lat_pollq\n\t"
    "s_mov_b32 s97, 149760\n\t"
    "s_call_b64 s[80:81], .Lat_init\n\t"
    "s_add_u32 s83, s27, 0\n\t"
    "s_add_u32 s96, s26, 2064384\n\t"
    "s_mov_b32 s94, 20\n\t"
    "s_call_b64 s[80:81], .Lat_a2\n\t"
    "s_mov_b32 s79, 0x1\n\t"
    "s_mov_b32 s99, 156928\n\t"
    "s_mov_b32 s56, 0\n\t"
    "s_call_b64 s[80:81], .Lat_merge\n\t"
    "s_mov_b32 s97, 148608\n\t"
    "s_call_b64 s[80:81], .Lat_init\n\t"
    "s_add_u32 s83, s27, 0\n\t"
    "s_mov_b32 s93, 9\n\t"
    "s_mov_b32 s94, 20\n\t"
    "s_mov_b32 s95, 18\n\t"
    "s_add_u32 s84, s27, 1548288\n\t"
    "s_add_u32 s85, s27, 1634304\n\t"
    "s_call_b64 s[80:81], .Lat_a1\n\t"
    "s_mov_b32 s82, 6\n\t"
    "s_add_u32 s83, s27, 1720320\n\t"
    "s_add_u32 s84, s25, 2666496\n\t"
    "s_add_u32 s85, s25, 2752512\n\t"
    "s_mov_b32 s86, 6\n\t"
    "s_mov_b32 s89, 6\n\t"
    "s_mov_b32 s90, 6\n\t"
    "s_mov_b32 s57, 0\n\t"
    "s_call_b64 s[80:81], .Lat_wl\n\t"
    "s_mov_b32 s79, 0x40000002\n\t"
    "s_mov_b32 s99, 160128\n\t"
    "s_mov_b32 s56, 0\n\t"
    "s_call_b64 s[80:81], .Lat_merge\n\t"
    "s_mov_b32 s97, 149376\n\t"
    "s_call_b64 s[80:81], .Lat_init\n\t"
    "s_add_i32 s82, s29, -7\n\t"
    "s_add_u32 s83, s25, 2838528\n\t"
    "s_add_u32 s84, s26, 2236416\n\t"
    "s_add_u32 s85, s26, 2322432\n\t"
    "s_mov_b32 s86, 0\n\t"
    "s_mov_b32 s89, 0\n\t"
    "s_mov_b32 s90, 0\n\t"
    "s_mov_b32 s57, 4\n\t"
    "s_call_b64 s[80:81], .Lat_wl\n\t"
    "s_mov_b32 s79, 0x20000002\n\t"
    "s_mov_b32 s99, 159328\n\t"
    "s_mov_b32 s56, 512\n\t"
    "s_call_b64 s[80:81], .Lat_merge\n\t"
    "s_mov_b32 s97, 149760\n\t"
    "s_call_b64 s[80:81], .Lat_init\n\t"
    "s_add_i32 s82, s30, -2\n\t"
    "s_add_u32 s83, s26, 2408448\n\t"
    "s_add_u32 s84, s27, 2064384\n\t"
    "s_add_u32 s85, s27, 2150400\n\t"
    "s_cmp_eq_u32 s68, 0\n\t"
    "s_cselect_b32 s84, 402653184, s84\n\t"
    "s_cselect_b32 s85, 402653184, s85\n\t"
    "s_mov_b32 s86, 0\n\t"
    "s_mov_b32 s89, 0\n\t"
    "s_mov_b32 s90, 0\n\t"
    "s_mov_b32 s57, 0\n\t"
    "s_call_b64 s[80:81], .Lat_wl\n\t"
    "s_mov_b32 s97, 3584\n\t"
    "s_call_b64 s[80:81], .Lat_prepoll\n\t"
    "s_mov_b32 s79, 0x10000002\n\t"
    "s_mov_b32 s99, 156928\n\t"
    "s_mov_b32 s56, 1024\n\t"
    "s_call_b64 s[80:81], .Lat_merge\n\t"
    "s_add_u32 s84, s27, 2064384\n\t"
    "s_add_u32 s85, s27, 2150400\n\t"
    "s_mov_b32 s97, 3584\n\t"
    "s_mov_b32 s61, 150144\n\t"
    "s_call_b64 s[80:81], .Lat_pollq\n\t"
    "s_mov_b32 s97, 150144\n\t"
    "s_call_b64 s[80:81], .Lat_init\n\t"
    "s_add_u32 s83, s27, 2236416\n\t"
    "s_add_u32 s96, s27, 2064384\n\t"
    "s_add_i32 s94, s31, -2\n\t"
    "s_call_b64 s[80:81], .Lat_a2\n\t"
    "s_add_u32 s83, s27, 2236416\n\t"
    "s_mov_b32 s93, 9\n\t"
    "s_add_i32 s94, s31, -2\n\t"
    "s_mov_b32 s95, s94\n\t"
    "s_mov_b32 s84, 402653184\n\t"
    "s_mov_b32 s85, 402653184\n\t"
    "s_call_b64 s[80:81], .Lat_a1\n\t"
    "s_mov_b32 s79, 0x50000003\n\t"
    "s_mov_b32 s99, 156928\n\t"
    "s_mov_b32 s56, 0\n\t"
    "s_call_b64 s[80:81], .Lat_merge\n\t"
    "s_endpgm\n\t"
    ".Lat_soft:\n\t"
    "s_nop 6\n\t"
    "v_mov_b32_dpp v0, v0 quad_perm:[0,0,0,0] row_mask:0xf bank_mask:0xf bound_ctrl:1\n\t"
    "v_mov_b32_dpp v2, v2 quad_perm:[2,2,2,2] row_mask:0xf bank_mask:0xf bound_ctrl:1\n\t"
    "s_nop 0\n\t"
    "v_add_f32_dpp v0, v1, v0 quad_perm:[1,1,1,1] row_mask:0xf bank_mask:0xf bound_ctrl:1\n\t"
    "v_add_f32_dpp v2, v3, v2 quad_perm:[3,3,3,3] row_mask:0xf bank_mask:0xf bound_ctrl:1\n\t"
    "v_add_f32 v2, v0, v2\n\t"
    "v_subrev_f32 v0, s60, v2\n\t"
    "v_cmp_lt_f32 vcc, 0, v0\n\t"
    "s_cbranch_vccnz .Lat_rare_8\n\t"
    ".Lat_back_9:\n\t"
    "v_exp_f32 v0, v0\n\t"
    "s_nop 0\n\t"
    "v_add_f32 v28, v28, v0\n\t"
    "v_mov_b32_dpp v1, v0 row_half_mirror row_mask:0xf bank_mask:0xf bound_ctrl:1\n\t"
    "v_cndmask_b32 v0, v1, v0, s[54:55]\n\t"
    "v_cndmask_b32 v1, 0, v0, s[52:53]\n\t"
    "v_cndmask_b32 v2, v0, 0, s[52:53]\n\t"
    "v_cvt_pk_bf16_f32 v1, v1, v2\n\t"
    "v_cndmask_b32 v0, 0, v1, s[44:45]\n\t"
    "v_cndmask_b32 v2, 0, v1, s[48:49]\n\t"
    "v_cndmask_b32 v3, 0, v1, s[50:51]\n\t"
    "v_cndmask_b32 v1, 0, v1, s[46:47]\n\t"
    "s_nop 0\n\t"
    "s_setpc_b64 s[58:59]\n\t"
    ".Lat_rare_8:\n\t"
    "v_max_f32_dpp v1, v2, v2 row_ror:4 row_mask:0xf bank_mask:0xf bound_ctrl:1\n\t"
    "s_nop 1\n\t"
    "v_max_f32_dpp v1, v1, v1 row_ror:8 row_mask:0xf bank_mask:0xf bound_ctrl:1\n\t"
    "s_nop 0\n\t"
    "v_readlane_b32 s61, v1, 0\n\t"
    "v_readlane_b32 s62, v1, 16\n\t"
    "s_nop 0\n\t"
    "v_mov_b32 v1, s61\n\t"
    "v_max_f32 v1, s62, v1\n\t"
    "v_sub_f32 v3, s60, v1\n\t"
    "v_sub_f32 v0, v2, v1\n\t"
    "v_exp_f32 v3, v3\n\t"
    "v_readfirstlane_b32 s60, v1\n\t"
    "v_mul_f32 v28, v28, v3\n\t"
    "v_mul_f32 v4, v4, v3\n\t"
    "v_mul_f32 v5, v5, v3\n\t"
    "v_mul_f32 v6, v6, v3\n\t"
    "v_mul_f32 v7, v7, v3\n\t"
    "v_mul_f32 v8, v8, v3\n\t"
    "v_mul_f32 v9, v9, v3\n\t"
    "v_mul_f32 v10, v10, v3\n\t"
    "v_mul_f32 v11, v11, v3\n\t"
    "v_mul_f32 v12, v12, v3\n\t"
    "v_mul_f32 v13, v13, v3\n\t"
    "v_mul_f32 v14, v14, v3\n\t"
    "v_mul_f32 v15, v15, v3\n\t"
    "s_branch .Lat_back_9\n\t"
    ".Lat_soft2:\n\t"
    "s_nop 6\n\t"
    "s_mov_b32 exec_lo, 0\n\t"
    "v_mov_b32 v0, v32\n\t"
    "v_mov_b32 v1, v33\n\t"
    "v_mov_b32 v2, v34\n\t"
    "v_mov_b32 v3, v35\n\t"
    "s_mov_b32 exec_lo, -1\n\t"
    "v_mov_b32_dpp v0, v0 quad_perm:[0,0,0,0] row_mask:0xf bank_mask:0xf bound_ctrl:1\n\t"
    "v_mov_b32_dpp v2, v2 quad_perm:[2,2,2,2] row_mask:0xf bank_mask:0xf bound_ctrl:1\n\t"
    "s_nop 0\n\t"
    "v_add_f32_dpp v0, v1, v0 quad_perm:[1,1,1,1] row_mask:0xf bank_mask:0xf bound_ctrl:1\n\t"
    "v_add_f32_dpp v2, v3, v2 quad_perm:[3,3,3,3] row_mask:0xf bank_mask:0xf bound_ctrl:1\n\t"
    "v_add_f32 v2, v0, v2\n\t"
    "v_subrev_f32 v0, s60, v2\n\t"
    "v_cmp_lt_f32 vcc, 0, v0\n\t"
    "s_cbranch_vccnz .Lat_rare_10\n\t"
    ".Lat_back_11:\n\t"
    "v_exp_f32 v0, v0\n\t"
    "s_nop 0\n\t"
    "v_cvt_pk_bf16_f32 v40, v0, v0\n\t"
    "v_fmac_f32 v28, 2.0, v0\n\t"
    "s_nop 0\n\t"
    "v_mov_b32_dpp v41, v40 row_half_mirror row_mask:0xf bank_mask:0xf bound_ctrl:1\n\t"
    "v_cndmask_b32 v42, v41, v40, s[32:33]\n\t"
    "v_cndmask_b32 v43, v40, v41, s[32:33]\n\t"
    "s_nop 1\n\t"
    "v_permlane32_swap_b32 v42, v43\n\t"
    "v_and_b32 v0, v42, v36\n\t"
    "v_and_b32 v1, v42, v37\n\t"
    "v_and_b32 v2, v42, v38\n\t"
    "v_and_b32 v3, v42, v39\n\t"
    "v_and_b32 v32, v43, v36\n\t"
    "v_and_b32 v33, v43, v37\n\t"
    "v_and_b32 v34, v43, v38\n\t"
    "v_and_b32 v35, v43, v39\n\t"
    "s_setpc_b64 s[58:59]\n\t"
    ".Lat_rare_10:\n\t"
    "v_max_f32_dpp v1, v2, v2 row_ror:4 row_mask:0xf bank_mask:0xf bound_ctrl:1\n\t"
    "s_nop 1\n\t"
    "v_max_f32_dpp v1, v1, v1 row_ror:8 row_mask:0xf bank_mask:0xf bound_ctrl:1\n\t"
    "s_nop 0\n\t"
    "v_readlane_b32 s61, v1, 0\n\t"
    "v_readlane_b32 s62, v1, 16\n\t"
    "v_readlane_b32 s63, v1, 32\n\t"
    "v_readlane_b32 vcc_lo, v1, 48\n\t"
    "v_mov_b32 v1, s61\n\t"
    "v_max_f32 v1, s62, v1\n\t"
    "v_max_f32 v1, s63, v1\n\t"
    "v_max_f32 v1, vcc_lo, v1\n\t"
    "v_sub_f32 v3, s60, v1\n\t"
    "v_sub_f32 v0, v2, v1\n\t"
    "v_exp_f32 v3, v3\n\t"
    "v_readfirstlane_b32 s60, v1\n\t"
    "v_mul_f32 v28, v28, v3\n\t"
    "v_mul_f32 v4, v4, v3\n\t"
    "v_mul_f32 v5, v5, v3\n\t"
    "v_mul_f32 v6, v6, v3\n\t"
    "v_mul_f32 v7, v7, v3\n\t"
    "v_mul_f32 v8, v8, v3\n\t"
    "v_mul_f32 v9, v9, v3\n\t"
    "v_mul_f32 v10, v10, v3\n\t"
    "v_mul_f32 v11, v11, v3\n\t"
    "v_mul_f32 v12, v12, v3\n\t"
    "v_mul_f32 v13, v13, v3\n\t"
    "v_mul_f32 v14, v14, v3\n\t"
    "v_mul_f32 v15, v15, v3\n\t"
    "s_branch .Lat_back_11\n\t"
    ".Lat_init:\n\t"
    "v_mov_b32 v4, 0\n\t"
    "v_mov_b32 v5, 0\n\t"
    "v_mov_b32 v6, 0\n\t"
    "v_mov_b32 v7, 0\n\t"
    "v_mov_b32 v8, 0\n\t"
    "v_mov_b32 v9, 0\n\t"
    "v_mov_b32 v10, 0\n\t"
    "v_mov_b32 v11, 0\n\t"
    "v_mov_b32 v12, 0\n\t"
    "v_mov_b32 v13, 0\n\t"
    "v_mov_b32 v14, 0\n\t"
    "v_mov_b32 v15, 0\n\t"
    "v_mov_b32 v16, 0\n\t"
    "v_mov_b32 v17, 0\n\t"
    "v_mov_b32 v18, 0\n\t"
    "v_mov_b32 v19, 0\n\t"
    "v_mov_b32 v20, 0\n\t"
    "v_mov_b32 v21, 0\n\t"
    "v_mov_b32 v22, 0\n\t"
    "v_mov_b32 v23, 0\n\t"
    "v_mov_b32 v24, 0\n\t"
    "v_mov_b32 v25, 0\n\t"
    "v_mov_b32 v26, 0\n\t"
    "v_mov_b32 v27, 0\n\t"
    "v_mov_b32 v28, 0\n\t"
    "s_mov_b32 s60, 0xff800000\n\t"
    "v_and_b32 v0, 255, v29\n\t"
    "s_sub_u32 s61, s97, 128\n\t"
    "v_add_u32 v0, s61, v0\n\t"
    "s_mov_b32 exec_lo, 0x0F0F0F0F\n\t"
    "s_mov_b32 exec_hi, 0x0F0F0F0F\n\t"
    "ds_read_b128 v[16:19], v0 offset:128\n\t"
    "s_mov_b32 exec_hi, 0\n\t"
    "ds_read_b128 v[20:23], v0 offset:384\n\t"
    "s_mov_b32 exec_lo, 0xF0F0F0F0\n\t"
    "s_mov_b32 exec_hi, 0xF0F0F0F0\n\t"
    "ds_read_b128 v[24:27], v0 offset:256\n\t"
    "s_mov_b32 exec_lo, 0\n\t"
    "ds_read_b128 v[20:23], v0\n\t"
    "s_mov_b64 exec, -1\n\t"
    "s_waitcnt lgkmcnt(0)\n\t"
    "s_setpc_b64 s[80:81]\n\t"
    ".Lat_prepoll:\n\t"
    "s_cmp_lg_u32 s68, 0\n\t"
    "s_cbranch_scc1 .Lat_ppw_12\n\t"
    "s_waitcnt vmcnt(0)\n\t"
    "v_mbcnt_lo_u32_b32 v32, -1, 0\n\t"
    "v_mbcnt_hi_u32_b32 v32, -1, v32\n\t"
    "v_lshlrev_b32 v32, 3, v32\n\t"
    "v_add_u32 v32, s97, v32\n\t"
    "global_load_dwordx2 v[34:35], v32, s[72:73] sc1\n\t"
    ".Lat_ppw_12:\n\t"
    "s_setpc_b64 s[80:81]\n\t"
    ".Lat_pollq:\n\t"
    "s_cmp_lg_u32 s68, 0\n\t"
    "s_cbranch_scc1 .Lat_pqw_13\n\t"
    "v_mbcnt_lo_u32_b32 v0, -1, 0\n\t"
    "v_mbcnt_hi_u32_b32 v0, -1, v0\n\t"
    "v_lshrrev_b32 v1, 4, v0\n\t"
    "v_mul_u32_u24 v1, 96, v1\n\t"
    "v_and_b32 v2, 15, v0\n\t"
    "v_lshl_add_u32 v1, v2, 1, v1\n\t"
    "v_lshlrev_b32 v0, 3, v0\n\t"
    "v_add_u32 v1, s61, v1\n\t"
    "s_mov_b32 s98, 0\n\t"
    "s_branch .Lat_pq_16\n\t"
    ".Lat_pq_14:\n\t"
    "global_load_dwordx2 v[34:35], v32, s[72:73] sc1\n\t"
    ".Lat_pq_16:\n\t"
    "s_waitcnt vmcnt(0)\n\t"
    "v_lshrrev_b32 v2, 16, v35\n\t"
    "v_cmp_eq_u32 vcc, 1, v2\n\t"
    "s_cmp_eq_u64 vcc, -1\n\t"
    "s_cbranch_scc1 .Lat_pq_15\n\t"
    "s_sleep 2\n\t"
    "s_add_u32 s98, s98, 1\n\t"
    "s_cmp_lt_u32 s98, 0x40000\n\t"
    "s_cbranch_scc1 .Lat_pq_14\n\t"
    "s_endpgm\n\t"
    ".Lat_pq_15:\n\t"
    "ds_write_b16 v1, v34 offset:0\n\t"
    "ds_write_b16_d16_hi v1, v34 offset:32\n\t"
    "ds_write_b16 v1, v35 offset:64\n\t"
    "s_waitcnt lgkmcnt(0)\n\t"
    "buffer_load_dwordx4 v[32:35], v29, s[36:39], s84 offen offset:0 nt\n\t"
    "buffer_load_dwordx4 v[36:39], v29, s[36:39], s84 offen offset:256 nt\n\t"
    "buffer_load_dwordx4 v[40:43], v29, s[36:39], s84 offen offset:512 nt\n\t"
    "buffer_load_dwordx4 v[44:47], v30, s[40:43], s84 offen nt\n\t"
    "buffer_load_dwordx4 v[48:51], v31, s[40:43], s84 offen nt\n\t"
    "buffer_load_dwordx4 v[52:55], v30, s[40:43], s84 offen offset:1664 nt\n\t"
    "buffer_load_dwordx4 v[56:59], v29, s[36:39], s85 offen offset:0 nt\n\t"
    "buffer_load_dwordx4 v[60:63], v29, s[36:39], s85 offen offset:256 nt\n\t"
    "buffer_load_dwordx4 v[64:67], v29, s[36:39], s85 offen offset:512 nt\n\t"
    "buffer_load_dwordx4 v[68:71], v30, s[40:43], s85 offen nt\n\t"
    "buffer_load_dwordx4 v[72:75], v31, s[40:43], s85 offen nt\n\t"
    "buffer_load_dwordx4 v[76:79], v30, s[40:43], s85 offen offset:1664 nt\n\t"
    ".Lat_pqw_13:\n\t"
    "s_barrier\n\t"
    "s_setpc_b64 s[80:81]\n\t"
    ".Lat_merge:\n\t"
    "s_nop 4\n\t"
    "v_mbcnt_lo_u32_b32 v0, -1, 0\n\t"
    "v_mbcnt_hi_u32_b32 v0, -1, v0\n\t"
    "s_mul_i32 s61, s68, 800\n\t"
    "s_add_u32 s61, s61, s77\n\t"
    "v_and_b32 v1, 15, v0\n\t"
    "v_lshlrev_b32 v1, 5, v1\n\t"
    "v_and_b32 v2, 16, v0\n\t"
    "v_add_u32 v1, v1, v2\n\t"
    "v_add_u32 v1, s61, v1\n\t"
    "v_subrev_u32 v3, 256, v1\n\t"
    "s_mov_b32 exec_hi, 0\n\t"
    "ds_write_b128 v1, v[4:7]\n\t"
    "s_mov_b32 exec_lo, 0x00FF00FF\n\t"
    "ds_write_b128 v1, v[8:11] offset:512\n\t"
    "s_mov_b32 exec_lo, 0\n\t"
    "s_mov_b32 exec_hi, -1\n\t"
    "ds_add_f32 v1, v12 offset:256\n\t"
    "ds_add_f32 v1, v13 offset:260\n\t"
    "ds_add_f32 v1, v14 offset:264\n\t"
    "ds_add_f32 v1, v15 offset:268\n\t"
    "s_mov_b32 exec_hi, 0xFF00FF00\n\t"
    "ds_add_f32 v3, v8 offset:0\n\t"
    "ds_add_f32 v3, v9 offset:4\n\t"
    "ds_add_f32 v3, v10 offset:8\n\t"
    "ds_add_f32 v3, v11 offset:12\n\t"
    "s_mov_b64 exec, -1\n\t"
    "v_add_f32_dpp v2, v28, v28 row_ror:4 row_mask:0xf bank_mask:0xf bound_ctrl:1\n\t"
    "s_nop 1\n\t"
    "v_add_f32_dpp v2, v2, v2 row_ror:8 row_mask:0xf bank_mask:0xf bound_ctrl:1\n\t"
    "v_mov_b32 v3, v2\n\t"
    "s_nop 1\n\t"
    "v_permlane32_swap_b32 v2, v3\n\t"
    "v_add_f32 v2, v2, v3\n\t"
    "v_mul_f32 v2, 0.5, v2\n\t"
    "v_lshrrev_b32 v3, 2, v0\n\t"
    "v_add_u32 v3, s61, v3\n\t"
    "v_mov_b32 v4, s60\n\t"
    "s_mov_b32 exec_lo, 0\n\t"
    "s_mov_b32 exec_hi, 1\n\t"
    "v_mov_b32 v2, v4\n\t"
    "s_mov_b32 exec_lo, 0x10001\n\t"
    "ds_write_b32 v3, v2 offset:768\n\t"
    "s_mov_b64 exec, -1\n\t"
    "s_waitcnt lgkmcnt(0)\n\t"
    "s_barrier\n\t"
    "s_cmp_lg_u32 s76, s68\n\t"
    "s_cbranch_scc1 .Lat_md_17\n\t"
    "v_lshlrev_b32 v1, 2, v0\n\t"
    "v_add_u32 v2, s99, v1\n\t"
    "v_add_u32 v1, s77, v1\n\t"
    "v_mov_b32 v3, s77\n\t"
    "v_mov_b32 v28, s99\n\t"
    "ds_read_b96 v[4:6], v3 offset:768\n\t"
    "ds_read_b96 v[8:10], v3 offset:1568\n\t"
    "ds_read_b96 v[12:14], v3 offset:2368\n\t"
    "ds_read_b96 v[16:18], v3 offset:3168\n\t"
    "s_bitcmp1_b32 s79, 0\n\t"
    "s_cbranch_scc1 .Lat_mf_18\n\t"
    "ds_read_b96 v[20:22], v28 offset:768\n\t"
    "ds_read_b32 v24, v2 offset:0\n\t"
    "ds_read_b32 v25, v2 offset:256\n\t"
    "ds_read_b32 v26, v2 offset:512\n\t"
    "s_branch .Lat_mg_19\n\t"
    ".Lat_mf_18:\n\t"
    "v_mov_b32 v20, 0\n\t"
    "v_mov_b32 v22, 0xff800000\n\t"
    "v_mov_b32 v24, 0\n\t"
    "v_mov_b32 v25, 0\n\t"
    "v_mov_b32 v26, 0\n\t"
    ".Lat_mg_19:\n\t"
    "s_waitcnt lgkmcnt(0)\n\t"
    "v_max3_f32 v23, v6, v10, v14\n\t"
    "v_max3_f32 v23, v23, v18, v22\n\t"
    "v_sub_f32 v6, v6, v23\n\t"
    "v_sub_f32 v10, v10, v23\n\t"
    "v_sub_f32 v14, v14, v23\n\t"
    "v_sub_f32 v18, v18, v23\n\t"
    "v_sub_f32 v22, v22, v23\n\t"
    "v_exp_f32 v6, v6\n\t"
    "v_exp_f32 v10, v10\n\t"
    "v_exp_f32 v14, v14\n\t"
    "v_exp_f32 v18, v18\n\t"
    "v_exp_f32 v22, v22\n\t"
    "v_add_f32 v4, v4, v5\n\t"
    "v_add_f32 v8, v8, v9\n\t"
    "v_add_f32 v12, v12, v13\n\t"
    "v_add_f32 v16, v16, v17\n\t"
    "v_mul_f32 v20, v20, v22\n\t"
    "v_fmac_f32 v20, v4, v6\n\t"
    "v_fmac_f32 v20, v8, v10\n\t"
    "v_fmac_f32 v20, v12, v14\n\t"
    "v_fmac_f32 v20, v16, v18\n\t"
    "v_mul_f32 v24, v24, v22\n\t"
    "v_mul_f32 v25, v25, v22\n\t"
    "v_mul_f32 v26, v26, v22\n\t"
    "ds_read_b32 v4, v1 offset:0\n\t"
    "ds_read_b32 v5, v1 offset:256\n\t"
    "ds_read_b32 v7, v1 offset:512\n\t"
    "ds_read_b32 v8, v1 offset:800\n\t"
    "ds_read_b32 v9, v1 offset:1056\n\t"
    "ds_read_b32 v11, v1 offset:1312\n\t"
    "ds_read_b32 v12, v1 offset:1600\n\t"
    "ds_read_b32 v13, v1 offset:1856\n\t"
    "ds_read_b32 v15, v1 offset:2112\n\t"
    "ds_read_b32 v16, v1 offset:2400\n\t"
    "ds_read_b32 v17, v1 offset:2656\n\t"
    "ds_read_b32 v19, v1 offset:2912\n\t"
    "s_waitcnt lgkmcnt(0)\n\t"
    "v_fmac_f32 v24, v4, v6\n\t"
    "v_fmac_f32 v25, v5, v6\n\t"
    "v_fmac_f32 v26, v7, v6\n\t"
    "v_fmac_f32 v24, v8, v10\n\t"
    "v_fmac_f32 v25, v9, v10\n\t"
    "v_fmac_f32 v26, v11, v10\n\t"
    "v_fmac_f32 v24, v12, v14\n\t"
    "v_fmac_f32 v25, v13, v14\n\t"
    "v_fmac_f32 v26, v15, v14\n\t"
    "v_fmac_f32 v24, v16, v18\n\t"
    "v_fmac_f32 v25, v17, v18\n\t"
    "v_fmac_f32 v26, v19, v18\n\t"
    "s_bitcmp1_b32 s79, 1\n\t"
    "s_cbranch_scc1 .Lat_mp_20\n\t"
    "ds_write_b32 v2, v24 offset:0\n\t"
    "ds_write_b32 v2, v25 offset:256\n\t"
    "ds_write_b32 v2, v26 offset:512\n\t"
    "v_mov_b32 v22, v23\n\t"
    "ds_write_b96 v28, v[20:22] offset:768\n\t"
    "s_branch .Lat_md_17\n\t"
    ".Lat_mp_20:\n\t"
    "v_rcp_f32 v4, v20\n\t"
    "v_log_f32 v5, v20\n\t"
    "s_nop 0\n\t"
    "v_add_f32 v5, v5, v23\n\t"
    "v_mul_f32 v24, v24, v4\n\t"
    "v_mul_f32 v25, v25, v4\n\t"
    "v_mul_f32 v26, v26, v4\n\t"
    "v_add_u32 v24, 0x800, v24\n\t"
    "v_lshrrev_b32 v24, 12, v24\n\t"
    "v_add_u32 v25, 0x800, v25\n\t"
    "v_lshrrev_b32 v25, 12, v25\n\t"
    "v_add_u32 v26, 0x800, v26\n\t"
    "v_lshrrev_b32 v26, 12, v26\n\t"
    "v_lshl_or_b32 v16, v25, 20, v24\n\t"
    "v_lshrrev_b32 v17, 12, v25\n\t"
    "v_lshl_or_b32 v17, v26, 8, v17\n\t"
    "v_and_b32 v6, 31, v0\n\t"
    "v_lshrrev_b32 v5, v6, v5\n\t"
    "v_lshl_or_b32 v17, v5, 31, v17\n\t"
    "s_and_b32 s61, s79, 0x70000000\n\t"
    "v_or_b32 v17, s61, v17\n\t"
    "v_lshlrev_b32 v1, 3, v0\n\t"
    "v_add_u32 v1, s56, v1\n\t"
    "global_store_dwordx2 v1, v[16:17], s[70:71] sc1\n\t"
    ".Lat_md_17:\n\t"
    "s_cmp_eq_u32 s76, 3\n\t"
    "s_cselect_b32 s76, 0, s76\n\t"
    "s_add_u32 s76, s76, 1\n\t"
    "s_xor_b32 s77, s77, 5248\n\t"
    "s_setpc_b64 s[80:81]\n\t"
    ".Lat_wl:\n\t"
    "s_mov_b32 s87, 0\n\t"
    "s_mov_b32 s91, 0\n\t"
    "s_cmp_eq_u32 s88, 1\n\t"
    "s_cbranch_scc1 .Lat_h1_22\n\t"
    ".Lat_h0_21:\n\t"
    "s_add_u32 s63, s87, 2\n\t"
    "s_cmp_eq_u32 s63, s82\n\t"
    "s_cselect_b32 s92, s84, s85\n\t"
    "s_cmp_lt_u32 s63, s82\n\t"
    "s_cselect_b32 s92, s83, s92\n\t"
    "s_add_u32 s83, s83, 86016\n\t"
    "s_cmp_ge_u32 s63, s89\n\t"
    "s_cselect_b32 s93, 1, 0\n\t"
    "s_cmp_lt_u32 s63, s90\n\t"
    "s_cselect_b32 s93, s93, 0\n\t"
    "s_waitcnt vmcnt(9)\n\t"
    "s_cmp_ge_u32 s87, s86\n\t"
    "s_cbranch_scc1 .Lat_nw_23\n\t"
    "v_add_u32 v0, s91, v29\n\t"
    "ds_write_b128 v0, v[32:35] offset:0\n\t"
    "ds_write_b128 v0, v[36:39] offset:256\n\t"
    "ds_write_b128 v0, v[40:43] offset:512\n\t"
    "s_waitcnt lgkmcnt(0)\n\t"
    ".Lat_nw_23:\n\t"
    "v_mfma_f32_16x16x32_bf16 v[0:3], v[16:19], v[32:35], 0\n\t"
    "v_mfma_f32_16x16x32_bf16 v[0:3], v[20:23], v[36:39], v[0:3]\n\t"
    "v_mfma_f32_16x16x32_bf16 v[0:3], v[24:27], v[40:43], v[0:3]\n\t"
    "s_cmp_ge_u32 s87, s57\n\t"
    "s_cbranch_scc1 .Lat_sl_24\n\t"
    "s_waitcnt vmcnt(3)\n\t"
    ".Lat_sl_24:\n\t"
    "s_cmp_eq_u32 s93, 1\n\t"
    "s_cbranch_scc1 .Lat_d_25\n\t"
    "buffer_load_dwordx4 v[32:35], v29, s[36:39], s92 offen offset:0 nt\n\t"
    "buffer_load_dwordx4 v[36:39], v29, s[36:39], s92 offen offset:256 nt\n\t"
    "buffer_load_dwordx4 v[40:43], v29, s[36:39], s92 offen offset:512 nt\n\t"
    "s_branch .Lat_e_26\n\t"
    ".Lat_d_25:\n\t"
    "buffer_load_dwordx4 v[32:35], v29, s[36:39], s92 offen offset:0\n\t"
    "buffer_load_dwordx4 v[36:39], v29, s[36:39], s92 offen offset:256\n\t"
    "buffer_load_dwordx4 v[40:43], v29, s[36:39], s92 offen offset:512\n\t"
    ".Lat_e_26:\n\t"
    "s_call_b64 s[58:59], .Lat_soft\n\t"
    "s_waitcnt vmcnt(9)\n\t"
    "v_mfma_f32_16x16x32_bf16 v[4:7], v[0:3], v[44:47], v[4:7]\n\t"
    "v_mfma_f32_16x16x32_bf16 v[8:11], v[0:3], v[48:51], v[8:11]\n\t"
    "v_mfma_f32_16x16x32_bf16 v[12:15], v[0:3], v[52:55], v[12:15]\n\t"
    "s_cmp_ge_u32 s87, s86\n\t"
    "s_cbranch_scc1 .Lat_nw_27\n\t"
    "v_add_u32 v0, s91, v29\n\t"
    "s_add_u32 s91, s91, 6144\n\t"
    "ds_write_b128 v0, v[44:47] offset:3072\n\t"
    "ds_write_b128 v0, v[48:51] offset:3328\n\t"
    "ds_write_b128 v0, v[52:55] offset:3584\n\t"
    "s_waitcnt lgkmcnt(0)\n\t"
    ".Lat_nw_27:\n\t"
    "s_cmp_ge_u32 s87, s57\n\t"
    "s_cbranch_scc1 .Lat_sl_28\n\t"
    "s_waitcnt vmcnt(3)\n\t"
    ".Lat_sl_28:\n\t"
    "s_cmp_eq_u32 s93, 1\n\t"
    "s_cbranch_scc1 .Lat_d_29\n\t"
    "buffer_load_dwordx4 v[44:47], v30, s[40:43], s92 offen nt\n\t"
    "buffer_load_dwordx4 v[48:51], v31, s[40:43], s92 offen nt\n\t"
    "buffer_load_dwordx4 v[52:55], v30, s[40:43], s92 offen offset:1664 nt\n\t"
    "s_branch .Lat_e_30\n\t"
    ".Lat_d_29:\n\t"
    "buffer_load_dwordx4 v[44:47], v30, s[40:43], s92 offen\n\t"
    "buffer_load_dwordx4 v[48:51], v31, s[40:43], s92 offen\n\t"
    "buffer_load_dwordx4 v[52:55], v30, s[40:43], s92 offen offset:1664\n\t"
    ".Lat_e_30:\n\t"
    "s_add_u32 s87, s87, 1\n\t"
    "s_mov_b32 s88, 1\n\t"
    "s_cmp_lt_u32 s87, s82\n\t"
    "s_cbranch_scc1 .Lat_h1_22\n\t"
    "s_setpc_b64 s[80:81]\n\t"
    ".Lat_h1_22:\n\t"
    "s_add_u32 s63, s87, 2\n\t"
    "s_cmp_eq_u32 s63, s82\n\t"
    "s_cselect_b32 s92, s84, s85\n\t"
    "s_cmp_lt_u32 s63, s82\n\t"
    "s_cselect_b32 s92, s83, s92\n\t"
    "s_add_u32 s83, s83, 86016\n\t"
    "s_cmp_ge_u32 s63, s89\n\t"
    "s_cselect_b32 s93, 1, 0\n\t"
    "s_cmp_lt_u32 s63, s90\n\t"
    "s_cselect_b32 s93, s93, 0\n\t"
    "s_waitcnt vmcnt(9)\n\t"
    "s_cmp_ge_u32 s87, s86\n\t"
    "s_cbranch_scc1 .Lat_nw_31\n\t"
    "v_add_u32 v0, s91, v29\n\t"
    "ds_write_b128 v0, v[56:59] offset:0\n\t"
    "ds_write_b128 v0, v[60:63] offset:256\n\t"
    "ds_write_b128 v0, v[64:67] offset:512\n\t"
    "s_waitcnt lgkmcnt(0)\n\t"
    ".Lat_nw_31:\n\t"
    "v_mfma_f32_16x16x32_bf16 v[0:3], v[16:19], v[56:59], 0\n\t"
    "v_mfma_f32_16x16x32_bf16 v[0:3], v[20:23], v[60:63], v[0:3]\n\t"
    "v_mfma_f32_16x16x32_bf16 v[0:3], v[24:27], v[64:67], v[0:3]\n\t"
    "s_cmp_ge_u32 s87, s57\n\t"
    "s_cbranch_scc1 .Lat_sl_32\n\t"
    "s_waitcnt vmcnt(3)\n\t"
    ".Lat_sl_32:\n\t"
    "s_cmp_eq_u32 s93, 1\n\t"
    "s_cbranch_scc1 .Lat_d_33\n\t"
    "buffer_load_dwordx4 v[56:59], v29, s[36:39], s92 offen offset:0 nt\n\t"
    "buffer_load_dwordx4 v[60:63], v29, s[36:39], s92 offen offset:256 nt\n\t"
    "buffer_load_dwordx4 v[64:67], v29, s[36:39], s92 offen offset:512 nt\n\t"
    "s_branch .Lat_e_34\n\t"
    ".Lat_d_33:\n\t"
    "buffer_load_dwordx4 v[56:59], v29, s[36:39], s92 offen offset:0\n\t"
    "buffer_load_dwordx4 v[60:63], v29, s[36:39], s92 offen offset:256\n\t"
    "buffer_load_dwordx4 v[64:67], v29, s[36:39], s92 offen offset:512\n\t"
    ".Lat_e_34:\n\t"
    "s_call_b64 s[58:59], .Lat_soft\n\t"
    "s_waitcnt vmcnt(9)\n\t"
    "v_mfma_f32_16x16x32_bf16 v[4:7], v[0:3], v[68:71], v[4:7]\n\t"
    "v_mfma_f32_16x16x32_bf16 v[8:11], v[0:3], v[72:75], v[8:11]\n\t"
    "v_mfma_f32_16x16x32_bf16 v[12:15], v[0:3], v[76:79], v[12:15]\n\t"
    "s_cmp_ge_u32 s87, s86\n\t"
    "s_cbranch_scc1 .Lat_nw_35\n\t"
    "v_add_u32 v0, s91, v29\n\t"
    "s_add_u32 s91, s91, 6144\n\t"
    "ds_write_b128 v0, v[68:71] offset:3072\n\t"
    "ds_write_b128 v0, v[72:75] offset:3328\n\t"
    "ds_write_b128 v0, v[76:79] offset:3584\n\t"
    "s_waitcnt lgkmcnt(0)\n\t"
    ".Lat_nw_35:\n\t"
    "s_cmp_ge_u32 s87, s57\n\t"
    "s_cbranch_scc1 .Lat_sl_36\n\t"
    "s_waitcnt vmcnt(3)\n\t"
    ".Lat_sl_36:\n\t"
    "s_cmp_eq_u32 s93, 1\n\t"
    "s_cbranch_scc1 .Lat_d_37\n\t"
    "buffer_load_dwordx4 v[68:71], v30, s[40:43], s92 offen nt\n\t"
    "buffer_load_dwordx4 v[72:75], v31, s[40:43], s92 offen nt\n\t"
    "buffer_load_dwordx4 v[76:79], v30, s[40:43], s92 offen offset:1664 nt\n\t"
    "s_branch .Lat_e_38\n\t"
    ".Lat_d_37:\n\t"
    "buffer_load_dwordx4 v[68:71], v30, s[40:43], s92 offen\n\t"
    "buffer_load_dwordx4 v[72:75], v31, s[40:43], s92 offen\n\t"
    "buffer_load_dwordx4 v[76:79], v30, s[40:43], s92 offen offset:1664\n\t"
    ".Lat_e_38:\n\t"
    "s_add_u32 s87, s87, 1\n\t"
    "s_mov_b32 s88, 0\n\t"
    "s_cmp_lt_u32 s87, s82\n\t"
    "s_cbranch_scc1 .Lat_h0_21\n\t"
    "s_setpc_b64 s[80:81]\n\t"
    ".Lat_a1:\n\t"
    "s_mov_b32 s88, 0\n\t"
    "s_cmp_eq_u32 s95, 0\n\t"
    "s_cbranch_scc0 .Lat_x_39\n\t"
    "s_setpc_b64 s[80:81]\n\t"
    ".Lat_x_39:\n\t"
    "s_waitcnt vmcnt(51)\n\t"
    "v_mfma_f32_16x16x32_bf16 v[0:3], v[16:19], v[80:83], 0\n\t"
    "v_mfma_f32_16x16x32_bf16 v[0:3], v[20:23], v[84:87], v[0:3]\n\t"
    "v_mfma_f32_16x16x32_bf16 v[0:3], v[24:27], v[88:91], v[0:3]\n\t"
    "s_call_b64 s[58:59], .Lat_soft\n\t"
    "s_waitcnt vmcnt(48)\n\t"
    "v_mfma_f32_16x16x32_bf16 v[4:7], v[0:3], v[92:95], v[4:7]\n\t"
    "v_mfma_f32_16x16x32_bf16 v[8:11], v[0:3], v[96:99], v[8:11]\n\t"
    "v_mfma_f32_16x16x32_bf16 v[12:15], v[0:3], v[100:103], v[12:15]\n\t"
    "s_cmp_eq_u32 s95, 1\n\t"
    "s_cbranch_scc0 .Lat_x_40\n\t"
    "s_setpc_b64 s[80:81]\n\t"
    ".Lat_x_40:\n\t"
    "s_cmp_lg_u32 s93, 16\n\t"
    "s_cbranch_scc1 .Lat_sk_41\n\t"
    "s_add_u32 s92, s83, 1376256\n\t"
    "s_cmp_gt_u32 s94, 16\n\t"
    "s_cselect_b32 s92, s92, 402653184\n\t"
    "buffer_load_dwordx4 a[208:211], v29, s[36:39], s92 offen offset:0 nt\n\t"
    "buffer_load_dwordx4 a[212:215], v29, s[36:39], s92 offen offset:256 nt\n\t"
    "buffer_load_dwordx4 a[216:219], v29, s[36:39], s92 offen offset:512 nt\n\t"
    "buffer_load_dwordx4 a[220:223], v30, s[40:43], s92 offen nt\n\t"
    "buffer_load_dwordx4 a[224:227], v31, s[40:43], s92 offen nt\n\t"
    "buffer_load_dwordx4 a[228:231], v30, s[40:43], s92 offen offset:1664 nt\n\t"
    ".Lat_sk_41:\n\t"
    "s_add_u32 s92, s83, 774144\n\t"
    "s_cmp_gt_u32 s94, 9\n\t"
    "s_cselect_b32 s92, s92, 402653184\n\t"
    "s_waitcnt vmcnt(45)\n\t"
    "v_mfma_f32_16x16x32_bf16 v[0:3], v[16:19], v[104:107], 0\n\t"
    "v_mfma_f32_16x16x32_bf16 v[0:3], v[20:23], v[108:111], v[0:3]\n\t"
    "v_mfma_f32_16x16x32_bf16 v[0:3], v[24:27], v[112:115], v[0:3]\n\t"
    "s_cmp_eq_u32 s93, 16\n\t"
    "s_cbranch_scc1 .Lat_sk_42\n\t"
    "buffer_load_dwordx4 a[40:43], v29, s[36:39], s92 offen offset:0 nt\n\t"
    "buffer_load_dwordx4 a[44:47], v29, s[36:39], s92 offen offset:256 nt\n\t"
    "buffer_load_dwordx4 a[48:51], v29, s[36:39], s92 offen offset:512 nt\n\t"
    ".Lat_sk_42:\n\t"
    "s_call_b64 s[58:59], .Lat_soft\n\t"
    "s_waitcnt vmcnt(45)\n\t"
    "v_mfma_f32_16x16x32_bf16 v[4:7], v[0:3], v[116:119], v[4:7]\n\t"
    "v_mfma_f32_16x16x32_bf16 v[8:11], v[0:3], v[120:123], v[8:11]\n\t"
    "v_mfma_f32_16x16x32_bf16 v[12:15], v[0:3], v[124:127], v[12:15]\n\t"
    "s_cmp_eq_u32 s93, 16\n\t"
    "s_cbranch_scc1 .Lat_sk_43\n\t"
    "buffer_load_dwordx4 a[52:55], v30, s[40:43], s92 offen nt\n\t"
    "buffer_load_dwordx4 a[56:59], v31, s[40:43], s92 offen nt\n\t"
    "buffer_load_dwordx4 a[60:63], v30, s[40:43], s92 offen offset:1664 nt\n\t"
    ".Lat_sk_43:\n\t"
    "s_cmp_eq_u32 s95, 2\n\t"
    "s_cbranch_scc0 .Lat_x_44\n\t"
    "s_setpc_b64 s[80:81]\n\t"
    ".Lat_x_44:\n\t"
    "s_waitcnt vmcnt(45)\n\t"
    "v_mfma_f32_16x16x32_bf16 v[0:3], v[16:19], v[128:131], 0\n\t"
    "v_mfma_f32_16x16x32_bf16 v[0:3], v[20:23], v[132:135], v[0:3]\n\t"
    "v_mfma_f32_16x16x32_bf16 v[0:3], v[24:27], v[136:139], v[0:3]\n\t"
    "s_call_b64 s[58:59], .Lat_soft\n\t"
    "s_waitcnt vmcnt(42)\n\t"
    "v_mfma_f32_16x16x32_bf16 v[4:7], v[0:3], v[140:143], v[4:7]\n\t"
    "v_mfma_f32_16x16x32_bf16 v[8:11], v[0:3], v[144:147], v[8:11]\n\t"
    "v_mfma_f32_16x16x32_bf16 v[12:15], v[0:3], v[148:151], v[12:15]\n\t"
    "s_cmp_eq_u32 s95, 3\n\t"
    "s_cbranch_scc0 .Lat_x_45\n\t"
    "s_setpc_b64 s[80:81]\n\t"
    ".Lat_x_45:\n\t"
    "s_waitcnt vmcnt(39)\n\t"
    "v_mfma_f32_16x16x32_bf16 v[0:3], v[16:19], v[152:155], 0\n\t"
    "v_mfma_f32_16x16x32_bf16 v[0:3], v[20:23], v[156:159], v[0:3]\n\t"
    "v_mfma_f32_16x16x32_bf16 v[0:3], v[24:27], v[160:163], v[0:3]\n\t"
    "s_call_b64 s[58:59], .Lat_soft\n\t"
    "s_waitcnt vmcnt(36)\n\t"
    "v_mfma_f32_16x16x32_bf16 v[4:7], v[0:3], v[164:167], v[4:7]\n\t"
    "v_mfma_f32_16x16x32_bf16 v[8:11], v[0:3], v[168:171], v[8:11]\n\t"
    "v_mfma_f32_16x16x32_bf16 v[12:15], v[0:3], v[172:175], v[12:15]\n\t"
    "s_cmp_eq_u32 s95, 4\n\t"
    "s_cbranch_scc0 .Lat_x_46\n\t"
    "s_setpc_b64 s[80:81]\n\t"
    ".Lat_x_46:\n\t"
    "s_waitcnt vmcnt(33)\n\t"
    "v_mfma_f32_16x16x32_bf16 v[0:3], v[16:19], v[176:179], 0\n\t"
    "v_mfma_f32_16x16x32_bf16 v[0:3], v[20:23], v[180:183], v[0:3]\n\t"
    "v_mfma_f32_16x16x32_bf16 v[0:3], v[24:27], v[184:187], v[0:3]\n\t"
    "s_call_b64 s[58:59], .Lat_soft\n\t"
    "s_waitcnt vmcnt(30)\n\t"
    "v_mfma_f32_16x16x32_bf16 v[4:7], v[0:3], v[188:191], v[4:7]\n\t"
    "v_mfma_f32_16x16x32_bf16 v[8:11], v[0:3], v[192:195], v[8:11]\n\t"
    "v_mfma_f32_16x16x32_bf16 v[12:15], v[0:3], v[196:199], v[12:15]\n\t"
    "s_cmp_eq_u32 s95, 5\n\t"
    "s_cbranch_scc0 .Lat_x_47\n\t"
    "s_setpc_b64 s[80:81]\n\t"
    ".Lat_x_47:\n\t"
    "s_cmp_lg_u32 s93, 16\n\t"
    "s_cbranch_scc1 .Lat_sk_48\n\t"
    "s_add_u32 s92, s83, 1462272\n\t"
    "s_cmp_gt_u32 s94, 17\n\t"
    "s_cselect_b32 s92, s92, 402653184\n\t"
    "buffer_load_dwordx4 a[232:235], v29, s[36:39], s92 offen offset:0 nt\n\t"
    "buffer_load_dwordx4 a[236:239], v29, s[36:39], s92 offen offset:256 nt\n\t"
    "buffer_load_dwordx4 a[240:243], v29, s[36:39], s92 offen offset:512 nt\n\t"
    "buffer_load_dwordx4 a[244:247], v30, s[40:43], s92 offen nt\n\t"
    "buffer_load_dwordx4 a[248:251], v31, s[40:43], s92 offen nt\n\t"
    "buffer_load_dwordx4 a[252:255], v30, s[40:43], s92 offen offset:1664 nt\n\t"
    ".Lat_sk_48:\n\t"
    "s_add_u32 s92, s83, 860160\n\t"
    "s_cmp_gt_u32 s94, 10\n\t"
    "s_cselect_b32 s92, s92, 402653184\n\t"
    "s_waitcnt vmcnt(27)\n\t"
    "v_mfma_f32_16x16x32_bf16 v[0:3], v[16:19], v[200:203], 0\n\t"
    "v_mfma_f32_16x16x32_bf16 v[0:3], v[20:23], v[204:207], v[0:3]\n\t"
    "v_mfma_f32_16x16x32_bf16 v[0:3], v[24:27], v[208:211], v[0:3]\n\t"
    "s_cmp_eq_u32 s93, 16\n\t"
    "s_cbranch_scc1 .Lat_sk_49\n\t"
    "buffer_load_dwordx4 a[64:67], v29, s[36:39], s92 offen offset:0 nt\n\t"
    "buffer_load_dwordx4 a[68:71], v29, s[36:39], s92 offen offset:256 nt\n\t"
    "buffer_load_dwordx4 a[72:75], v29, s[36:39], s92 offen offset:512 nt\n\t"
    ".Lat_sk_49:\n\t"
    "s_call_b64 s[58:59], .Lat_soft\n\t"
    "s_waitcnt vmcnt(27)\n\t"
    "v_mfma_f32_16x16x32_bf16 v[4:7], v[0:3], v[212:215], v[4:7]\n\t"
    "v_mfma_f32_16x16x32_bf16 v[8:11], v[0:3], v[216:219], v[8:11]\n\t"
    "v_mfma_f32_16x16x32_bf16 v[12:15], v[0:3], v[220:223], v[12:15]\n\t"
    "s_cmp_eq_u32 s93, 16\n\t"
    "s_cbranch_scc1 .Lat_sk_50\n\t"
    "buffer_load_dwordx4 a[76:79], v30, s[40:43], s92 offen nt\n\t"
    "buffer_load_dwordx4 a[80:83], v31, s[40:43], s92 offen nt\n\t"
    "buffer_load_dwordx4 a[84:87], v30, s[40:43], s92 offen offset:1664 nt\n\t"
    ".Lat_sk_50:\n\t"
    "s_cmp_eq_u32 s95, 6\n\t"
    "s_cbranch_scc0 .Lat_x_51\n\t"
    "s_setpc_b64 s[80:81]\n\t"
    ".Lat_x_51:\n\t"
    "s_waitcnt vmcnt(27)\n\t"
    "v_mfma_f32_16x16x32_bf16 v[0:3], v[16:19], v[224:227], 0\n\t"
    "v_mfma_f32_16x16x32_bf16 v[0:3], v[20:23], v[228:231], v[0:3]\n\t"
    "v_mfma_f32_16x16x32_bf16 v[0:3], v[24:27], v[232:235], v[0:3]\n\t"
    "s_call_b64 s[58:59], .Lat_soft\n\t"
    "s_waitcnt vmcnt(24)\n\t"
    "v_mfma_f32_16x16x32_bf16 v[4:7], v[0:3], v[236:239], v[4:7]\n\t"
    "v_mfma_f32_16x16x32_bf16 v[8:11], v[0:3], v[240:243], v[8:11]\n\t"
    "v_mfma_f32_16x16x32_bf16 v[12:15], v[0:3], v[244:247], v[12:15]\n\t"
    "s_cmp_eq_u32 s95, 7\n\t"
    "s_cbranch_scc0 .Lat_x_52\n\t"
    "s_setpc_b64 s[80:81]\n\t"
    ".Lat_x_52:\n\t"
    "s_waitcnt vmcnt(21)\n\t"
    "v_mfma_f32_16x16x32_bf16 v[0:3], v[16:19], v[248:251], 0\n\t"
    "v_mfma_f32_16x16x32_bf16 v[0:3], v[20:23], v[252:255], v[0:3]\n\t"
    "v_mfma_f32_16x16x32_bf16 v[0:3], v[24:27], a[0:3], v[0:3]\n\t"
    "s_call_b64 s[58:59], .Lat_soft\n\t"
    "s_waitcnt vmcnt(18)\n\t"
    "v_mfma_f32_16x16x32_bf16 v[4:7], v[0:3], a[4:7], v[4:7]\n\t"
    "v_mfma_f32_16x16x32_bf16 v[8:11], v[0:3], a[8:11], v[8:11]\n\t"
    "v_mfma_f32_16x16x32_bf16 v[12:15], v[0:3], a[12:15], v[12:15]\n\t"
    "s_cmp_eq_u32 s95, 8\n\t"
    "s_cbranch_scc0 .Lat_x_53\n\t"
    "s_setpc_b64 s[80:81]\n\t"
    ".Lat_x_53:\n\t"
    "s_waitcnt vmcnt(15)\n\t"
    "v_mfma_f32_16x16x32_bf16 v[0:3], v[16:19], a[16:19], 0\n\t"
    "v_mfma_f32_16x16x32_bf16 v[0:3], v[20:23], a[20:23], v[0:3]\n\t"
    "v_mfma_f32_16x16x32_bf16 v[0:3], v[24:27], a[24:27], v[0:3]\n\t"
    "s_call_b64 s[58:59], .Lat_soft\n\t"
    "s_waitcnt vmcnt(12)\n\t"
    "v_mfma_f32_16x16x32_bf16 v[4:7], v[0:3], a[28:31], v[4:7]\n\t"
    "v_mfma_f32_16x16x32_bf16 v[8:11], v[0:3], a[32:35], v[8:11]\n\t"
    "v_mfma_f32_16x16x32_bf16 v[12:15], v[0:3], a[36:39], v[12:15]\n\t"
    "s_cmp_eq_u32 s95, 9\n\t"
    "s_cbranch_scc0 .Lat_x_54\n\t"
    "s_setpc_b64 s[80:81]\n\t"
    ".Lat_x_54:\n\t"
    "s_cmp_lg_u32 s93, 16\n\t"
    "s_cbranch_scc1 .Lat_sk_55\n\t"
    "s_mov_b32 s92, s84\n\t"
    "s_cmp_gt_u32 s94, 18\n\t"
    "s_cselect_b32 s92, s92, 402653184\n\t"
    "buffer_load_dwordx4 v[32:35], v29, s[36:39], s92 offen offset:0 nt\n\t"
    "buffer_load_dwordx4 v[36:39], v29, s[36:39], s92 offen offset:256 nt\n\t"
    "buffer_load_dwordx4 v[40:43], v29, s[36:39], s92 offen offset:512 nt\n\t"
    "buffer_load_dwordx4 v[44:47], v30, s[40:43], s92 offen nt\n\t"
    "buffer_load_dwordx4 v[48:51], v31, s[40:43], s92 offen nt\n\t"
    "buffer_load_dwordx4 v[52:55], v30, s[40:43], s92 offen offset:1664 nt\n\t"
    ".Lat_sk_55:\n\t"
    "s_add_u32 s92, s83, 946176\n\t"
    "s_cmp_gt_u32 s94, 11\n\t"
    "s_cselect_b32 s92, s92, 402653184\n\t"
    "s_waitcnt vmcnt(9)\n\t"
    "v_mfma_f32_16x16x32_bf16 v[0:3], v[16:19], a[40:43], 0\n\t"
    "v_mfma_f32_16x16x32_bf16 v[0:3], v[20:23], a[44:47], v[0:3]\n\t"
    "v_mfma_f32_16x16x32_bf16 v[0:3], v[24:27], a[48:51], v[0:3]\n\t"
    "s_cmp_eq_u32 s93, 16\n\t"
    "s_cbranch_scc1 .Lat_sk_56\n\t"
    "buffer_load_dwordx4 a[88:91], v29, s[36:39], s92 offen offset:0 nt\n\t"
    "buffer_load_dwordx4 a[92:95], v29, s[36:39], s92 offen offset:256 nt\n\t"
    "buffer_load_dwordx4 a[96:99], v29, s[36:39], s92 offen offset:512 nt\n\t"
    ".Lat_sk_56:\n\t"
    "s_call_b64 s[58:59], .Lat_soft\n\t"
    "s_waitcnt vmcnt(9)\n\t"
    "v_mfma_f32_16x16x32_bf16 v[4:7], v[0:3], a[52:55], v[4:7]\n\t"
    "v_mfma_f32_16x16x32_bf16 v[8:11], v[0:3], a[56:59], v[8:11]\n\t"
    "v_mfma_f32_16x16x32_bf16 v[12:15], v[0:3], a[60:63], v[12:15]\n\t"
    "s_cmp_eq_u32 s93, 16\n\t"
    "s_cbranch_scc1 .Lat_sk_57\n\t"
    "buffer_load_dwordx4 a[100:103], v30, s[40:43], s92 offen nt\n\t"
    "buffer_load_dwordx4 a[104:107], v31, s[40:43], s92 offen nt\n\t"
    "buffer_load_dwordx4 a[108:111], v30, s[40:43], s92 offen offset:1664 nt\n\t"
    ".Lat_sk_57:\n\t"
    "s_cmp_eq_u32 s95, 10\n\t"
    "s_cbranch_scc0 .Lat_x_58\n\t"
    "s_setpc_b64 s[80:81]\n\t"
    ".Lat_x_58:\n\t"
    "s_add_u32 s92, s83, 1032192\n\t"
    "s_cmp_gt_u32 s94, 12\n\t"
    "s_cselect_b32 s92, s92, 402653184\n\t"
    "s_waitcnt vmcnt(9)\n\t"
    "v_mfma_f32_16x16x32_bf16 v[0:3], v[16:19], a[64:67], 0\n\t"
    "v_mfma_f32_16x16x32_bf16 v[0:3], v[20:23], a[68:71], v[0:3]\n\t"
    "v_mfma_f32_16x16x32_bf16 v[0:3], v[24:27], a[72:75], v[0:3]\n\t"
    "s_cmp_eq_u32 s93, 16\n\t"
    "s_cbranch_scc1 .Lat_sk_59\n\t"
    "buffer_load_dwordx4 a[112:115], v29, s[36:39], s92 offen offset:0 nt\n\t"
    "buffer_load_dwordx4 a[116:119], v29, s[36:39], s92 offen offset:256 nt\n\t"
    "buffer_load_dwordx4 a[120:123], v29, s[36:39], s92 offen offset:512 nt\n\t"
    ".Lat_sk_59:\n\t"
    "s_call_b64 s[58:59], .Lat_soft\n\t"
    "s_waitcnt vmcnt(9)\n\t"
    "v_mfma_f32_16x16x32_bf16 v[4:7], v[0:3], a[76:79], v[4:7]\n\t"
    "v_mfma_f32_16x16x32_bf16 v[8:11], v[0:3], a[80:83], v[8:11]\n\t"
    "v_mfma_f32_16x16x32_bf16 v[12:15], v[0:3], a[84:87], v[12:15]\n\t"
    "s_cmp_eq_u32 s93, 16\n\t"
    "s_cbranch_scc1 .Lat_sk_60\n\t"
    "buffer_load_dwordx4 a[124:127], v30, s[40:43], s92 offen nt\n\t"
    "buffer_load_dwordx4 a[128:131], v31, s[40:43], s92 offen nt\n\t"
    "buffer_load_dwordx4 a[132:135], v30, s[40:43], s92 offen offset:1664 nt\n\t"
    ".Lat_sk_60:\n\t"
    "s_cmp_eq_u32 s95, 11\n\t"
    "s_cbranch_scc0 .Lat_x_61\n\t"
    "s_setpc_b64 s[80:81]\n\t"
    ".Lat_x_61:\n\t"
    "s_add_u32 s92, s83, 1118208\n\t"
    "s_cmp_gt_u32 s94, 13\n\t"
    "s_cselect_b32 s92, s92, 402653184\n\t"
    "s_waitcnt vmcnt(9)\n\t"
    "v_mfma_f32_16x16x32_bf16 v[0:3], v[16:19], a[88:91], 0\n\t"
    "v_mfma_f32_16x16x32_bf16 v[0:3], v[20:23], a[92:95], v[0:3]\n\t"
    "v_mfma_f32_16x16x32_bf16 v[0:3], v[24:27], a[96:99], v[0:3]\n\t"
    "s_cmp_eq_u32 s93, 16\n\t"
    "s_cbranch_scc1 .Lat_sk_62\n\t"
    "buffer_load_dwordx4 a[136:139], v29, s[36:39], s92 offen offset:0 nt\n\t"
    "buffer_load_dwordx4 a[140:143], v29, s[36:39], s92 offen offset:256 nt\n\t"
    "buffer_load_dwordx4 a[144:147], v29, s[36:39], s92 offen offset:512 nt\n\t"
    ".Lat_sk_62:\n\t"
    "s_call_b64 s[58:59], .Lat_soft\n\t"
    "s_waitcnt vmcnt(9)\n\t"
    "v_mfma_f32_16x16x32_bf16 v[4:7], v[0:3], a[100:103], v[4:7]\n\t"
    "v_mfma_f32_16x16x32_bf16 v[8:11], v[0:3], a[104:107], v[8:11]\n\t"
    "v_mfma_f32_16x16x32_bf16 v[12:15], v[0:3], a[108:111], v[12:15]\n\t"
    "s_cmp_eq_u32 s93, 16\n\t"
    "s_cbranch_scc1 .Lat_sk_63\n\t"
    "buffer_load_dwordx4 a[148:151], v30, s[40:43], s92 offen nt\n\t"
    "buffer_load_dwordx4 a[152:155], v31, s[40:43], s92 offen nt\n\t"
    "buffer_load_dwordx4 a[156:159], v30, s[40:43], s92 offen offset:1664 nt\n\t"
    ".Lat_sk_63:\n\t"
    "s_cmp_eq_u32 s95, 12\n\t"
    "s_cbranch_scc0 .Lat_x_64\n\t"
    "s_setpc_b64 s[80:81]\n\t"
    ".Lat_x_64:\n\t"
    "s_add_u32 s92, s83, 1204224\n\t"
    "s_cmp_gt_u32 s94, 14\n\t"
    "s_cselect_b32 s92, s92, 402653184\n\t"
    "s_waitcnt vmcnt(9)\n\t"
    "v_mfma_f32_16x16x32_bf16 v[0:3], v[16:19], a[112:115], 0\n\t"
    "v_mfma_f32_16x16x32_bf16 v[0:3], v[20:23], a[116:119], v[0:3]\n\t"
    "v_mfma_f32_16x16x32_bf16 v[0:3], v[24:27], a[120:123], v[0:3]\n\t"
    "s_cmp_eq_u32 s93, 16\n\t"
    "s_cbranch_scc1 .Lat_sk_65\n\t"
    "buffer_load_dwordx4 a[160:163], v29, s[36:39], s92 offen offset:0 nt\n\t"
    "buffer_load_dwordx4 a[164:167], v29, s[36:39], s92 offen offset:256 nt\n\t"
    "buffer_load_dwordx4 a[168:171], v29, s[36:39], s92 offen offset:512 nt\n\t"
    ".Lat_sk_65:\n\t"
    "s_call_b64 s[58:59], .Lat_soft\n\t"
    "s_waitcnt vmcnt(9)\n\t"
    "v_mfma_f32_16x16x32_bf16 v[4:7], v[0:3], a[124:127], v[4:7]\n\t"
    "v_mfma_f32_16x16x32_bf16 v[8:11], v[0:3], a[128:131], v[8:11]\n\t"
    "v_mfma_f32_16x16x32_bf16 v[12:15], v[0:3], a[132:135], v[12:15]\n\t"
    "s_cmp_eq_u32 s93, 16\n\t"
    "s_cbranch_scc1 .Lat_sk_66\n\t"
    "buffer_load_dwordx4 a[172:175], v30, s[40:43], s92 offen nt\n\t"
    "buffer_load_dwordx4 a[176:179], v31, s[40:43], s92 offen nt\n\t"
    "buffer_load_dwordx4 a[180:183], v30, s[40:43], s92 offen offset:1664 nt\n\t"
    ".Lat_sk_66:\n\t"
    "s_cmp_eq_u32 s95, 13\n\t"
    "s_cbranch_scc0 .Lat_x_67\n\t"
    "s_setpc_b64 s[80:81]\n\t"
    ".Lat_x_67:\n\t"
    "s_cmp_lg_u32 s93, 16\n\t"
    "s_cbranch_scc1 .Lat_sk_68\n\t"
    "s_mov_b32 s92, s85\n\t"
    "s_cmp_gt_u32 s94, 19\n\t"
    "s_cselect_b32 s92, s92, 402653184\n\t"
    "buffer_load_dwordx4 v[56:59], v29, s[36:39], s92 offen offset:0 nt\n\t"
    "buffer_load_dwordx4 v[60:63], v29, s[36:39], s92 offen offset:256 nt\n\t"
    "buffer_load_dwordx4 v[64:67], v29, s[36:39], s92 offen offset:512 nt\n\t"
    "buffer_load_dwordx4 v[68:71], v30, s[40:43], s92 offen nt\n\t"
    "buffer_load_dwordx4 v[72:75], v31, s[40:43], s92 offen nt\n\t"
    "buffer_load_dwordx4 v[76:79], v30, s[40:43], s92 offen offset:1664 nt\n\t"
    ".Lat_sk_68:\n\t"
    "s_add_u32 s92, s83, 1290240\n\t"
    "s_cmp_gt_u32 s94, 15\n\t"
    "s_cselect_b32 s92, s92, 402653184\n\t"
    "s_waitcnt vmcnt(9)\n\t"
    "v_mfma_f32_16x16x32_bf16 v[0:3], v[16:19], a[136:139], 0\n\t"
    "v_mfma_f32_16x16x32_bf16 v[0:3], v[20:23], a[140:143], v[0:3]\n\t"
    "v_mfma_f32_16x16x32_bf16 v[0:3], v[24:27], a[144:147], v[0:3]\n\t"
    "s_cmp_eq_u32 s93, 16\n\t"
    "s_cbranch_scc1 .Lat_sk_69\n\t"
    "buffer_load_dwordx4 a[184:187], v29, s[36:39], s92 offen offset:0 nt\n\t"
    "buffer_load_dwordx4 a[188:191], v29, s[36:39], s92 offen offset:256 nt\n\t"
    "buffer_load_dwordx4 a[192:195], v29, s[36:39], s92 offen offset:512 nt\n\t"
    ".Lat_sk_69:\n\t"
    "s_call_b64 s[58:59], .Lat_soft\n\t"
    "s_waitcnt vmcnt(9)\n\t"
    "v_mfma_f32_16x16x32_bf16 v[4:7], v[0:3], a[148:151], v[4:7]\n\t"
    "v_mfma_f32_16x16x32_bf16 v[8:11], v[0:3], a[152:155], v[8:11]\n\t"
    "v_mfma_f32_16x16x32_bf16 v[12:15], v[0:3], a[156:159], v[12:15]\n\t"
    "s_cmp_eq_u32 s93, 16\n\t"
    "s_cbranch_scc1 .Lat_sk_70\n\t"
    "buffer_load_dwordx4 a[196:199], v30, s[40:43], s92 offen nt\n\t"
    "buffer_load_dwordx4 a[200:203], v31, s[40:43], s92 offen nt\n\t"
    "buffer_load_dwordx4 a[204:207], v30, s[40:43], s92 offen offset:1664 nt\n\t"
    ".Lat_sk_70:\n\t"
    "s_cmp_eq_u32 s95, 14\n\t"
    "s_cbranch_scc0 .Lat_x_71\n\t"
    "s_setpc_b64 s[80:81]\n\t"
    ".Lat_x_71:\n\t"
    "s_add_u32 s92, s83, 1376256\n\t"
    "s_cmp_gt_u32 s94, 16\n\t"
    "s_cselect_b32 s92, s92, 402653184\n\t"
    "s_waitcnt vmcnt(9)\n\t"
    "v_mfma_f32_16x16x32_bf16 v[0:3], v[16:19], a[160:163], 0\n\t"
    "v_mfma_f32_16x16x32_bf16 v[0:3], v[20:23], a[164:167], v[0:3]\n\t"
    "v_mfma_f32_16x16x32_bf16 v[0:3], v[24:27], a[168:171], v[0:3]\n\t"
    "s_cmp_eq_u32 s93, 16\n\t"
    "s_cbranch_scc1 .Lat_sk_72\n\t"
    "buffer_load_dwordx4 a[208:211], v29, s[36:39], s92 offen offset:0 nt\n\t"
    "buffer_load_dwordx4 a[212:215], v29, s[36:39], s92 offen offset:256 nt\n\t"
    "buffer_load_dwordx4 a[216:219], v29, s[36:39], s92 offen offset:512 nt\n\t"
    ".Lat_sk_72:\n\t"
    "s_call_b64 s[58:59], .Lat_soft\n\t"
    "s_waitcnt vmcnt(9)\n\t"
    "v_mfma_f32_16x16x32_bf16 v[4:7], v[0:3], a[172:175], v[4:7]\n\t"
    "v_mfma_f32_16x16x32_bf16 v[8:11], v[0:3], a[176:179], v[8:11]\n\t"
    "v_mfma_f32_16x16x32_bf16 v[12:15], v[0:3], a[180:183], v[12:15]\n\t"
    "s_cmp_eq_u32 s93, 16\n\t"
    "s_cbranch_scc1 .Lat_sk_73\n\t"
    "buffer_load_dwordx4 a[220:223], v30, s[40:43], s92 offen nt\n\t"
    "buffer_load_dwordx4 a[224:227], v31, s[40:43], s92 offen nt\n\t"
    "buffer_load_dwordx4 a[228:231], v30, s[40:43], s92 offen offset:1664 nt\n\t"
    ".Lat_sk_73:\n\t"
    "s_cmp_eq_u32 s95, 15\n\t"
    "s_cbranch_scc0 .Lat_x_74\n\t"
    "s_setpc_b64 s[80:81]\n\t"
    ".Lat_x_74:\n\t"
    "s_add_u32 s92, s83, 1462272\n\t"
    "s_cmp_gt_u32 s94, 17\n\t"
    "s_cselect_b32 s92, s92, 402653184\n\t"
    "s_waitcnt vmcnt(9)\n\t"
    "v_mfma_f32_16x16x32_bf16 v[0:3], v[16:19], a[184:187], 0\n\t"
    "v_mfma_f32_16x16x32_bf16 v[0:3], v[20:23], a[188:191], v[0:3]\n\t"
    "v_mfma_f32_16x16x32_bf16 v[0:3], v[24:27], a[192:195], v[0:3]\n\t"
    "s_cmp_eq_u32 s93, 16\n\t"
    "s_cbranch_scc1 .Lat_sk_75\n\t"
    "buffer_load_dwordx4 a[232:235], v29, s[36:39], s92 offen offset:0 nt\n\t"
    "buffer_load_dwordx4 a[236:239], v29, s[36:39], s92 offen offset:256 nt\n\t"
    "buffer_load_dwordx4 a[240:243], v29, s[36:39], s92 offen offset:512 nt\n\t"
    ".Lat_sk_75:\n\t"
    "s_call_b64 s[58:59], .Lat_soft\n\t"
    "s_waitcnt vmcnt(9)\n\t"
    "v_mfma_f32_16x16x32_bf16 v[4:7], v[0:3], a[196:199], v[4:7]\n\t"
    "v_mfma_f32_16x16x32_bf16 v[8:11], v[0:3], a[200:203], v[8:11]\n\t"
    "v_mfma_f32_16x16x32_bf16 v[12:15], v[0:3], a[204:207], v[12:15]\n\t"
    "s_cmp_eq_u32 s93, 16\n\t"
    "s_cbranch_scc1 .Lat_sk_76\n\t"
    "buffer_load_dwordx4 a[244:247], v30, s[40:43], s92 offen nt\n\t"
    "buffer_load_dwordx4 a[248:251], v31, s[40:43], s92 offen nt\n\t"
    "buffer_load_dwordx4 a[252:255], v30, s[40:43], s92 offen offset:1664 nt\n\t"
    ".Lat_sk_76:\n\t"
    "s_cmp_eq_u32 s95, 16\n\t"
    "s_cbranch_scc0 .Lat_x_77\n\t"
    "s_setpc_b64 s[80:81]\n\t"
    ".Lat_x_77:\n\t"
    "s_mov_b32 s92, s84\n\t"
    "s_cmp_gt_u32 s94, 18\n\t"
    "s_cselect_b32 s92, s92, 402653184\n\t"
    "s_waitcnt vmcnt(9)\n\t"
    "v_mfma_f32_16x16x32_bf16 v[0:3], v[16:19], a[208:211], 0\n\t"
    "v_mfma_f32_16x16x32_bf16 v[0:3], v[20:23], a[212:215], v[0:3]\n\t"
    "v_mfma_f32_16x16x32_bf16 v[0:3], v[24:27], a[216:219], v[0:3]\n\t"
    "s_cmp_eq_u32 s93, 16\n\t"
    "s_cbranch_scc1 .Lat_sk_78\n\t"
    "buffer_load_dwordx4 v[32:35], v29, s[36:39], s92 offen offset:0 nt\n\t"
    "buffer_load_dwordx4 v[36:39], v29, s[36:39], s92 offen offset:256 nt\n\t"
    "buffer_load_dwordx4 v[40:43], v29, s[36:39], s92 offen offset:512 nt\n\t"
    ".Lat_sk_78:\n\t"
    "s_call_b64 s[58:59], .Lat_soft\n\t"
    "s_waitcnt vmcnt(9)\n\t"
    "v_mfma_f32_16x16x32_bf16 v[4:7], v[0:3], a[220:223], v[4:7]\n\t"
    "v_mfma_f32_16x16x32_bf16 v[8:11], v[0:3], a[224:227], v[8:11]\n\t"
    "v_mfma_f32_16x16x32_bf16 v[12:15], v[0:3], a[228:231], v[12:15]\n\t"
    "s_cmp_eq_u32 s93, 16\n\t"
    "s_cbranch_scc1 .Lat_sk_79\n\t"
    "buffer_load_dwordx4 v[44:47], v30, s[40:43], s92 offen nt\n\t"
    "buffer_load_dwordx4 v[48:51], v31, s[40:43], s92 offen nt\n\t"
    "buffer_load_dwordx4 v[52:55], v30, s[40:43], s92 offen offset:1664 nt\n\t"
    ".Lat_sk_79:\n\t"
    "s_cmp_eq_u32 s95, 17\n\t"
    "s_cbranch_scc0 .Lat_x_80\n\t"
    "s_setpc_b64 s[80:81]\n\t"
    ".Lat_x_80:\n\t"
    "s_mov_b32 s92, s85\n\t"
    "s_cmp_gt_u32 s94, 19\n\t"
    "s_cselect_b32 s92, s92, 402653184\n\t"
    "s_waitcnt vmcnt(9)\n\t"
    "v_mfma_f32_16x16x32_bf16 v[0:3], v[16:19], a[232:235], 0\n\t"
    "v_mfma_f32_16x16x32_bf16 v[0:3], v[20:23], a[236:239], v[0:3]\n\t"
    "v_mfma_f32_16x16x32_bf16 v[0:3], v[24:27], a[240:243], v[0:3]\n\t"
    "s_cmp_eq_u32 s93, 16\n\t"
    "s_cbranch_scc1 .Lat_sk_81\n\t"
    "buffer_load_dwordx4 v[56:59], v29, s[36:39], s92 offen offset:0 nt\n\t"
    "buffer_load_dwordx4 v[60:63], v29, s[36:39], s92 offen offset:256 nt\n\t"
    "buffer_load_dwordx4 v[64:67], v29, s[36:39], s92 offen offset:512 nt\n\t"
    ".Lat_sk_81:\n\t"
    "s_call_b64 s[58:59], .Lat_soft\n\t"
    "s_waitcnt vmcnt(9)\n\t"
    "v_mfma_f32_16x16x32_bf16 v[4:7], v[0:3], a[244:247], v[4:7]\n\t"
    "v_mfma_f32_16x16x32_bf16 v[8:11], v[0:3], a[248:251], v[8:11]\n\t"
    "v_mfma_f32_16x16x32_bf16 v[12:15], v[0:3], a[252:255], v[12:15]\n\t"
    "s_cmp_eq_u32 s93, 16\n\t"
    "s_cbranch_scc1 .Lat_sk_82\n\t"
    "buffer_load_dwordx4 v[68:71], v30, s[40:43], s92 offen nt\n\t"
    "buffer_load_dwordx4 v[72:75], v31, s[40:43], s92 offen nt\n\t"
    "buffer_load_dwordx4 v[76:79], v30, s[40:43], s92 offen offset:1664 nt\n\t"
    ".Lat_sk_82:\n\t"
    "s_setpc_b64 s[80:81]\n\t"
    ".Lat_a2:\n\t"
    "v_mfma_f32_16x16x32_bf16 v[0:3], v[16:19], v[80:83], 0\n\t"
    "v_mfma_f32_16x16x32_bf16 v[0:3], v[20:23], v[84:87], v[0:3]\n\t"
    "v_mfma_f32_16x16x32_bf16 v[0:3], v[24:27], v[88:91], v[0:3]\n\t"
    "s_call_b64 s[58:59], .Lat_soft\n\t"
    "v_mfma_f32_16x16x32_bf16 v[4:7], v[0:3], v[92:95], v[4:7]\n\t"
    "v_mfma_f32_16x16x32_bf16 v[8:11], v[0:3], v[96:99], v[8:11]\n\t"
    "v_mfma_f32_16x16x32_bf16 v[12:15], v[0:3], v[100:103], v[12:15]\n\t"
    "s_add_u32 s92, s83, 0\n\t"
    "s_cmp_gt_u32 s94, 0\n\t"
    "s_cselect_b32 s92, s92, 402653184\n\t"
    "buffer_load_dwordx4 v[80:83], v29, s[36:39], s92 offen offset:0 nt\n\t"
    "buffer_load_dwordx4 v[84:87], v29, s[36:39], s92 offen offset:256 nt\n\t"
    "buffer_load_dwordx4 v[88:91], v29, s[36:39], s92 offen offset:512 nt\n\t"
    "buffer_load_dwordx4 v[92:95], v30, s[40:43], s92 offen nt\n\t"
    "buffer_load_dwordx4 v[96:99], v31, s[40:43], s92 offen nt\n\t"
    "buffer_load_dwordx4 v[100:103], v30, s[40:43], s92 offen offset:1664 nt\n\t"
    "v_mfma_f32_16x16x32_bf16 v[0:3], v[16:19], v[104:107], 0\n\t"
    "v_mfma_f32_16x16x32_bf16 v[0:3], v[20:23], v[108:111], v[0:3]\n\t"
    "v_mfma_f32_16x16x32_bf16 v[0:3], v[24:27], v[112:115], v[0:3]\n\t"
    "s_call_b64 s[58:59], .Lat_soft\n\t"
    "v_mfma_f32_16x16x32_bf16 v[4:7], v[0:3], v[116:119], v[4:7]\n\t"
    "v_mfma_f32_16x16x32_bf16 v[8:11], v[0:3], v[120:123], v[8:11]\n\t"
    "v_mfma_f32_16x16x32_bf16 v[12:15], v[0:3], v[124:127], v[12:15]\n\t"
    "s_add_u32 s92, s83, 86016\n\t"
    "s_cmp_gt_u32 s94, 1\n\t"
    "s_cselect_b32 s92, s92, 402653184\n\t"
    "buffer_load_dwordx4 v[104:107], v29, s[36:39], s92 offen offset:0 nt\n\t"
    "buffer_load_dwordx4 v[108:111], v29, s[36:39], s92 offen offset:256 nt\n\t"
    "buffer_load_dwordx4 v[112:115], v29, s[36:39], s92 offen offset:512 nt\n\t"
    "buffer_load_dwordx4 v[116:119], v30, s[40:43], s92 offen nt\n\t"
    "buffer_load_dwordx4 v[120:123], v31, s[40:43], s92 offen nt\n\t"
    "buffer_load_dwordx4 v[124:127], v30, s[40:43], s92 offen offset:1664 nt\n\t"
    "v_mfma_f32_16x16x32_bf16 v[0:3], v[16:19], a[208:211], 0\n\t"
    "v_mfma_f32_16x16x32_bf16 v[0:3], v[20:23], a[212:215], v[0:3]\n\t"
    "v_mfma_f32_16x16x32_bf16 v[0:3], v[24:27], a[216:219], v[0:3]\n\t"
    "s_call_b64 s[58:59], .Lat_soft\n\t"
    "v_mfma_f32_16x16x32_bf16 v[4:7], v[0:3], a[220:223], v[4:7]\n\t"
    "v_mfma_f32_16x16x32_bf16 v[8:11], v[0:3], a[224:227], v[8:11]\n\t"
    "v_mfma_f32_16x16x32_bf16 v[12:15], v[0:3], a[228:231], v[12:15]\n\t"
    "ds_read_b128 a[208:211], v29 offset:0\n\t"
    "ds_read_b128 a[212:215], v29 offset:256\n\t"
    "ds_read_b128 a[216:219], v29 offset:512\n\t"
    "ds_read_b128 a[220:223], v29 offset:3072\n\t"
    "ds_read_b128 a[224:227], v29 offset:3328\n\t"
    "ds_read_b128 a[228:231], v29 offset:3584\n\t"
    "v_mfma_f32_16x16x32_bf16 v[0:3], v[16:19], a[232:235], 0\n\t"
    "v_mfma_f32_16x16x32_bf16 v[0:3], v[20:23], a[236:239], v[0:3]\n\t"
    "v_mfma_f32_16x16x32_bf16 v[0:3], v[24:27], a[240:243], v[0:3]\n\t"
    "s_call_b64 s[58:59], .Lat_soft\n\t"
    "v_mfma_f32_16x16x32_bf16 v[4:7], v[0:3], a[244:247], v[4:7]\n\t"
    "v_mfma_f32_16x16x32_bf16 v[8:11], v[0:3], a[248:251], v[8:11]\n\t"
    "v_mfma_f32_16x16x32_bf16 v[12:15], v[0:3], a[252:255], v[12:15]\n\t"
    "ds_read_b128 a[232:235], v29 offset:6144\n\t"
    "ds_read_b128 a[236:239], v29 offset:6400\n\t"
    "ds_read_b128 a[240:243], v29 offset:6656\n\t"
    "ds_read_b128 a[244:247], v29 offset:9216\n\t"
    "ds_read_b128 a[248:251], v29 offset:9472\n\t"
    "ds_read_b128 a[252:255], v29 offset:9728\n\t"
    "v_mfma_f32_16x16x32_bf16 v[0:3], v[16:19], v[128:131], 0\n\t"
    "v_mfma_f32_16x16x32_bf16 v[0:3], v[20:23], v[132:135], v[0:3]\n\t"
    "v_mfma_f32_16x16x32_bf16 v[0:3], v[24:27], v[136:139], v[0:3]\n\t"
    "s_call_b64 s[58:59], .Lat_soft\n\t"
    "v_mfma_f32_16x16x32_bf16 v[4:7], v[0:3], v[140:143], v[4:7]\n\t"
    "v_mfma_f32_16x16x32_bf16 v[8:11], v[0:3], v[144:147], v[8:11]\n\t"
    "v_mfma_f32_16x16x32_bf16 v[12:15], v[0:3], v[148:151], v[12:15]\n\t"
    "s_waitcnt vmcnt(12)\n\t"
    "v_mfma_f32_16x16x32_bf16 v[0:3], v[16:19], v[32:35], 0\n\t"
    "v_mfma_f32_16x16x32_bf16 v[0:3], v[20:23], v[36:39], v[0:3]\n\t"
    "v_mfma_f32_16x16x32_bf16 v[0:3], v[24:27], v[40:43], v[0:3]\n\t"
    "s_call_b64 s[58:59], .Lat_soft\n\t"
    "v_mfma_f32_16x16x32_bf16 v[4:7], v[0:3], v[44:47], v[4:7]\n\t"
    "v_mfma_f32_16x16x32_bf16 v[8:11], v[0:3], v[48:51], v[8:11]\n\t"
    "v_mfma_f32_16x16x32_bf16 v[12:15], v[0:3], v[52:55], v[12:15]\n\t"
    "s_add_u32 s92, s83, 172032\n\t"
    "s_cmp_gt_u32 s94, 2\n\t"
    "s_cselect_b32 s92, s92, 402653184\n\t"
    "buffer_load_dwordx4 v[128:131], v29, s[36:39], s92 offen offset:0 nt\n\t"
    "buffer_load_dwordx4 v[132:135], v29, s[36:39], s92 offen offset:256 nt\n\t"
    "buffer_load_dwordx4 v[136:139], v29, s[36:39], s92 offen offset:512 nt\n\t"
    "buffer_load_dwordx4 v[140:143], v30, s[40:43], s92 offen nt\n\t"
    "buffer_load_dwordx4 v[144:147], v31, s[40:43], s92 offen nt\n\t"
    "buffer_load_dwordx4 v[148:151], v30, s[40:43], s92 offen offset:1664 nt\n\t"
    "v_mfma_f32_16x16x32_bf16 v[0:3], v[16:19], v[56:59], 0\n\t"
    "v_mfma_f32_16x16x32_bf16 v[0:3], v[20:23], v[60:63], v[0:3]\n\t"
    "v_mfma_f32_16x16x32_bf16 v[0:3], v[24:27], v[64:67], v[0:3]\n\t"
    "s_call_b64 s[58:59], .Lat_soft\n\t"
    "v_mfma_f32_16x16x32_bf16 v[4:7], v[0:3], v[68:71], v[4:7]\n\t"
    "v_mfma_f32_16x16x32_bf16 v[8:11], v[0:3], v[72:75], v[8:11]\n\t"
    "v_mfma_f32_16x16x32_bf16 v[12:15], v[0:3], v[76:79], v[12:15]\n\t"
    "s_waitcnt lgkmcnt(0)\n\t"
    "v_mfma_f32_16x16x32_bf16 v[0:3], v[16:19], a[208:211], 0\n\t"
    "v_mfma_f32_16x16x32_bf16 v[0:3], v[20:23], a[212:215], v[0:3]\n\t"
    "v_mfma_f32_16x16x32_bf16 v[0:3], v[24:27], a[216:219], v[0:3]\n\t"
    "s_call_b64 s[58:59], .Lat_soft\n\t"
    "v_mfma_f32_16x16x32_bf16 v[4:7], v[0:3], a[220:223], v[4:7]\n\t"
    "v_mfma_f32_16x16x32_bf16 v[8:11], v[0:3], a[224:227], v[8:11]\n\t"
    "v_mfma_f32_16x16x32_bf16 v[12:15], v[0:3], a[228:231], v[12:15]\n\t"
    "ds_read_b128 a[208:211], v29 offset:12288\n\t"
    "ds_read_b128 a[212:215], v29 offset:12544\n\t"
    "ds_read_b128 a[216:219], v29 offset:12800\n\t"
    "ds_read_b128 a[220:223], v29 offset:15360\n\t"
    "ds_read_b128 a[224:227], v29 offset:15616\n\t"
    "ds_read_b128 a[228:231], v29 offset:15872\n\t"
    "v_mfma_f32_16x16x32_bf16 v[0:3], v[16:19], a[232:235], 0\n\t"
    "v_mfma_f32_16x16x32_bf16 v[0:3], v[20:23], a[236:239], v[0:3]\n\t"
    "v_mfma_f32_16x16x32_bf16 v[0:3], v[24:27], a[240:243], v[0:3]\n\t"
    "s_call_b64 s[58:59], .Lat_soft\n\t"
    "v_mfma_f32_16x16x32_bf16 v[4:7], v[0:3], a[244:247], v[4:7]\n\t"
    "v_mfma_f32_16x16x32_bf16 v[8:11], v[0:3], a[248:251], v[8:11]\n\t"
    "v_mfma_f32_16x16x32_bf16 v[12:15], v[0:3], a[252:255], v[12:15]\n\t"
    "ds_read_b128 a[232:235], v29 offset:18432\n\t"
    "ds_read_b128 a[236:239], v29 offset:18688\n\t"
    "ds_read_b128 a[240:243], v29 offset:18944\n\t"
    "ds_read_b128 a[244:247], v29 offset:21504\n\t"
    "ds_read_b128 a[248:251], v29 offset:21760\n\t"
    "ds_read_b128 a[252:255], v29 offset:22016\n\t"
    "v_mfma_f32_16x16x32_bf16 v[0:3], v[16:19], v[152:155], 0\n\t"
    "v_mfma_f32_16x16x32_bf16 v[0:3], v[20:23], v[156:159], v[0:3]\n\t"
    "v_mfma_f32_16x16x32_bf16 v[0:3], v[24:27], v[160:163], v[0:3]\n\t"
    "s_call_b64 s[58:59], .Lat_soft\n\t"
    "v_mfma_f32_16x16x32_bf16 v[4:7], v[0:3], v[164:167], v[4:7]\n\t"
    "v_mfma_f32_16x16x32_bf16 v[8:11], v[0:3], v[168:171], v[8:11]\n\t"
    "v_mfma_f32_16x16x32_bf16 v[12:15], v[0:3], v[172:175], v[12:15]\n\t"
    "s_add_u32 s92, s83, 258048\n\t"
    "s_cmp_gt_u32 s94, 3\n\t"
    "s_cselect_b32 s92, s92, 402653184\n\t"
    "buffer_load_dwordx4 v[152:155], v29, s[36:39], s92 offen offset:0 nt\n\t"
    "buffer_load_dwordx4 v[156:159], v29, s[36:39], s92 offen offset:256 nt\n\t"
    "buffer_load_dwordx4 v[160:163], v29, s[36:39], s92 offen offset:512 nt\n\t"
    "buffer_load_dwordx4 v[164:167], v30, s[40:43], s92 offen nt\n\t"
    "buffer_load_dwordx4 v[168:171], v31, s[40:43], s92 offen nt\n\t"
    "buffer_load_dwordx4 v[172:175], v30, s[40:43], s92 offen offset:1664 nt\n\t"
    "v_mfma_f32_16x16x32_bf16 v[0:3], v[16:19], v[176:179], 0\n\t"
    "v_mfma_f32_16x16x32_bf16 v[0:3], v[20:23], v[180:183], v[0:3]\n\t"
    "v_mfma_f32_16x16x32_bf16 v[0:3], v[24:27], v[184:187], v[0:3]\n\t"
    "s_call_b64 s[58:59], .Lat_soft\n\t"
    "v_mfma_f32_16x16x32_bf16 v[4:7], v[0:3], v[188:191], v[4:7]\n\t"
    "v_mfma_f32_16x16x32_bf16 v[8:11], v[0:3], v[192:195], v[8:11]\n\t"
    "v_mfma_f32_16x16x32_bf16 v[12:15], v[0:3], v[196:199], v[12:15]\n\t"
    "s_waitcnt lgkmcnt(0)\n\t"
    "v_mfma_f32_16x16x32_bf16 v[0:3], v[16:19], a[208:211], 0\n\t"
    "v_mfma_f32_16x16x32_bf16 v[0:3], v[20:23], a[212:215], v[0:3]\n\t"
    "v_mfma_f32_16x16x32_bf16 v[0:3], v[24:27], a[216:219], v[0:3]\n\t"
    "s_call_b64 s[58:59], .Lat_soft\n\t"
    "v_mfma_f32_16x16x32_bf16 v[4:7], v[0:3], a[220:223], v[4:7]\n\t"
    "v_mfma_f32_16x16x32_bf16 v[8:11], v[0:3], a[224:227], v[8:11]\n\t"
    "v_mfma_f32_16x16x32_bf16 v[12:15], v[0:3], a[228:231], v[12:15]\n\t"
    "s_add_u32 s92, s83, 344064\n\t"
    "s_cmp_gt_u32 s94, 4\n\t"
    "s_cselect_b32 s92, s92, 402653184\n\t"
    "buffer_load_dwordx4 v[176:179], v29, s[36:39], s92 offen offset:0 nt\n\t"
    "buffer_load_dwordx4 v[180:183], v29, s[36:39], s92 offen offset:256 nt\n\t"
    "buffer_load_dwordx4 v[184:187], v29, s[36:39], s92 offen offset:512 nt\n\t"
    "buffer_load_dwordx4 v[188:191], v30, s[40:43], s92 offen nt\n\t"
    "buffer_load_dwordx4 v[192:195], v31, s[40:43], s92 offen nt\n\t"
    "buffer_load_dwordx4 v[196:199], v30, s[40:43], s92 offen offset:1664 nt\n\t"
    "ds_read_b128 a[208:211], v29 offset:24576\n\t"
    "ds_read_b128 a[212:215], v29 offset:24832\n\t"
    "ds_read_b128 a[216:219], v29 offset:25088\n\t"
    "ds_read_b128 a[220:223], v29 offset:27648\n\t"
    "ds_read_b128 a[224:227], v29 offset:27904\n\t"
    "ds_read_b128 a[228:231], v29 offset:28160\n\t"
    "v_mfma_f32_16x16x32_bf16 v[0:3], v[16:19], a[232:235], 0\n\t"
    "v_mfma_f32_16x16x32_bf16 v[0:3], v[20:23], a[236:239], v[0:3]\n\t"
    "v_mfma_f32_16x16x32_bf16 v[0:3], v[24:27], a[240:243], v[0:3]\n\t"
    "s_call_b64 s[58:59], .Lat_soft\n\t"
    "v_mfma_f32_16x16x32_bf16 v[4:7], v[0:3], a[244:247], v[4:7]\n\t"
    "v_mfma_f32_16x16x32_bf16 v[8:11], v[0:3], a[248:251], v[8:11]\n\t"
    "v_mfma_f32_16x16x32_bf16 v[12:15], v[0:3], a[252:255], v[12:15]\n\t"
    "ds_read_b128 a[232:235], v29 offset:30720\n\t"
    "ds_read_b128 a[236:239], v29 offset:30976\n\t"
    "ds_read_b128 a[240:243], v29 offset:31232\n\t"
    "ds_read_b128 a[244:247], v29 offset:33792\n\t"
    "ds_read_b128 a[248:251], v29 offset:34048\n\t"
    "ds_read_b128 a[252:255], v29 offset:34304\n\t"
    "v_mfma_f32_16x16x32_bf16 v[0:3], v[16:19], v[200:203], 0\n\t"
    "v_mfma_f32_16x16x32_bf16 v[0:3], v[20:23], v[204:207], v[0:3]\n\t"
    "v_mfma_f32_16x16x32_bf16 v[0:3], v[24:27], v[208:211], v[0:3]\n\t"
    "s_call_b64 s[58:59], .Lat_soft\n\t"
    "v_mfma_f32_16x16x32_bf16 v[4:7], v[0:3], v[212:215], v[4:7]\n\t"
    "v_mfma_f32_16x16x32_bf16 v[8:11], v[0:3], v[216:219], v[8:11]\n\t"
    "v_mfma_f32_16x16x32_bf16 v[12:15], v[0:3], v[220:223], v[12:15]\n\t"
    "v_mfma_f32_16x16x32_bf16 v[0:3], v[16:19], v[224:227], 0\n\t"
    "v_mfma_f32_16x16x32_bf16 v[0:3], v[20:23], v[228:231], v[0:3]\n\t"
    "v_mfma_f32_16x16x32_bf16 v[0:3], v[24:27], v[232:235], v[0:3]\n\t"
    "s_call_b64 s[58:59], .Lat_soft\n\t"
    "v_mfma_f32_16x16x32_bf16 v[4:7], v[0:3], v[236:239], v[4:7]\n\t"
    "v_mfma_f32_16x16x32_bf16 v[8:11], v[0:3], v[240:243], v[8:11]\n\t"
    "v_mfma_f32_16x16x32_bf16 v[12:15], v[0:3], v[244:247], v[12:15]\n\t"
    "s_waitcnt lgkmcnt(0)\n\t"
    "v_mfma_f32_16x16x32_bf16 v[0:3], v[16:19], a[208:211], 0\n\t"
    "v_mfma_f32_16x16x32_bf16 v[0:3], v[20:23], a[212:215], v[0:3]\n\t"
    "v_mfma_f32_16x16x32_bf16 v[0:3], v[24:27], a[216:219], v[0:3]\n\t"
    "s_call_b64 s[58:59], .Lat_soft\n\t"
    "v_mfma_f32_16x16x32_bf16 v[4:7], v[0:3], a[220:223], v[4:7]\n\t"
    "v_mfma_f32_16x16x32_bf16 v[8:11], v[0:3], a[224:227], v[8:11]\n\t"
    "v_mfma_f32_16x16x32_bf16 v[12:15], v[0:3], a[228:231], v[12:15]\n\t"
    "s_add_u32 s92, s83, 430080\n\t"
    "s_cmp_gt_u32 s94, 5\n\t"
    "s_cselect_b32 s92, s92, 402653184\n\t"
    "buffer_load_dwordx4 v[200:203], v29, s[36:39], s92 offen offset:0 nt\n\t"
    "buffer_load_dwordx4 v[204:207], v29, s[36:39], s92 offen offset:256 nt\n\t"
    "buffer_load_dwordx4 v[208:211], v29, s[36:39], s92 offen offset:512 nt\n\t"
    "buffer_load_dwordx4 v[212:215], v30, s[40:43], s92 offen nt\n\t"
    "buffer_load_dwordx4 v[216:219], v31, s[40:43], s92 offen nt\n\t"
    "buffer_load_dwordx4 v[220:223], v30, s[40:43], s92 offen offset:1664 nt\n\t"
    "v_mfma_f32_16x16x32_bf16 v[0:3], v[16:19], a[232:235], 0\n\t"
    "v_mfma_f32_16x16x32_bf16 v[0:3], v[20:23], a[236:239], v[0:3]\n\t"
    "v_mfma_f32_16x16x32_bf16 v[0:3], v[24:27], a[240:243], v[0:3]\n\t"
    "s_call_b64 s[58:59], .Lat_soft\n\t"
    "v_mfma_f32_16x16x32_bf16 v[4:7], v[0:3], a[244:247], v[4:7]\n\t"
    "v_mfma_f32_16x16x32_bf16 v[8:11], v[0:3], a[248:251], v[8:11]\n\t"
    "v_mfma_f32_16x16x32_bf16 v[12:15], v[0:3], a[252:255], v[12:15]\n\t"
    "v_mfma_f32_16x16x32_bf16 v[0:3], v[16:19], v[248:251], 0\n\t"
    "v_mfma_f32_16x16x32_bf16 v[0:3], v[20:23], v[252:255], v[0:3]\n\t"
    "v_mfma_f32_16x16x32_bf16 v[0:3], v[24:27], a[0:3], v[0:3]\n\t"
    "s_call_b64 s[58:59], .Lat_soft\n\t"
    "v_mfma_f32_16x16x32_bf16 v[4:7], v[0:3], a[4:7], v[4:7]\n\t"
    "v_mfma_f32_16x16x32_bf16 v[8:11], v[0:3], a[8:11], v[8:11]\n\t"
    "v_mfma_f32_16x16x32_bf16 v[12:15], v[0:3], a[12:15], v[12:15]\n\t"
    "v_mfma_f32_16x16x32_bf16 v[0:3], v[16:19], a[16:19], 0\n\t"
    "v_mfma_f32_16x16x32_bf16 v[0:3], v[20:23], a[20:23], v[0:3]\n\t"
    "v_mfma_f32_16x16x32_bf16 v[0:3], v[24:27], a[24:27], v[0:3]\n\t"
    "s_call_b64 s[58:59], .Lat_soft\n\t"
    "v_mfma_f32_16x16x32_bf16 v[4:7], v[0:3], a[28:31], v[4:7]\n\t"
    "v_mfma_f32_16x16x32_bf16 v[8:11], v[0:3], a[32:35], v[8:11]\n\t"
    "v_mfma_f32_16x16x32_bf16 v[12:15], v[0:3], a[36:39], v[12:15]\n\t"
    "s_add_u32 s92, s83, 516096\n\t"
    "s_cmp_gt_u32 s94, 6\n\t"
    "s_cselect_b32 s92, s92, 402653184\n\t"
    "buffer_load_dwordx4 v[224:227], v29, s[36:39], s92 offen offset:0 nt\n\t"
    "buffer_load_dwordx4 v[228:231], v29, s[36:39], s92 offen offset:256 nt\n\t"
    "buffer_load_dwordx4 v[232:235], v29, s[36:39], s92 offen offset:512 nt\n\t"
    "buffer_load_dwordx4 v[236:239], v30, s[40:43], s92 offen nt\n\t"
    "buffer_load_dwordx4 v[240:243], v31, s[40:43], s92 offen nt\n\t"
    "buffer_load_dwordx4 v[244:247], v30, s[40:43], s92 offen offset:1664 nt\n\t"
    "v_mfma_f32_16x16x32_bf16 v[0:3], v[16:19], a[40:43], 0\n\t"
    "v_mfma_f32_16x16x32_bf16 v[0:3], v[20:23], a[44:47], v[0:3]\n\t"
    "v_mfma_f32_16x16x32_bf16 v[0:3], v[24:27], a[48:51], v[0:3]\n\t"
    "s_call_b64 s[58:59], .Lat_soft\n\t"
    "v_mfma_f32_16x16x32_bf16 v[4:7], v[0:3], a[52:55], v[4:7]\n\t"
    "v_mfma_f32_16x16x32_bf16 v[8:11], v[0:3], a[56:59], v[8:11]\n\t"
    "v_mfma_f32_16x16x32_bf16 v[12:15], v[0:3], a[60:63], v[12:15]\n\t"
    "v_mfma_f32_16x16x32_bf16 v[0:3], v[16:19], a[64:67], 0\n\t"
    "v_mfma_f32_16x16x32_bf16 v[0:3], v[20:23], a[68:71], v[0:3]\n\t"
    "v_mfma_f32_16x16x32_bf16 v[0:3], v[24:27], a[72:75], v[0:3]\n\t"
    "s_call_b64 s[58:59], .Lat_soft\n\t"
    "v_mfma_f32_16x16x32_bf16 v[4:7], v[0:3], a[76:79], v[4:7]\n\t"
    "v_mfma_f32_16x16x32_bf16 v[8:11], v[0:3], a[80:83], v[8:11]\n\t"
    "v_mfma_f32_16x16x32_bf16 v[12:15], v[0:3], a[84:87], v[12:15]\n\t"
    "v_mfma_f32_16x16x32_bf16 v[0:3], v[16:19], a[88:91], 0\n\t"
    "v_mfma_f32_16x16x32_bf16 v[0:3], v[20:23], a[92:95], v[0:3]\n\t"
    "v_mfma_f32_16x16x32_bf16 v[0:3], v[24:27], a[96:99], v[0:3]\n\t"
    "s_call_b64 s[58:59], .Lat_soft\n\t"
    "v_mfma_f32_16x16x32_bf16 v[4:7], v[0:3], a[100:103], v[4:7]\n\t"
    "v_mfma_f32_16x16x32_bf16 v[8:11], v[0:3], a[104:107], v[8:11]\n\t"
    "v_mfma_f32_16x16x32_bf16 v[12:15], v[0:3], a[108:111], v[12:15]\n\t"
    "s_add_u32 s92, s83, 602112\n\t"
    "s_cmp_gt_u32 s94, 7\n\t"
    "s_cselect_b32 s92, s92, 402653184\n\t"
    "buffer_load_dwordx4 v[248:251], v29, s[36:39], s92 offen offset:0 nt\n\t"
    "buffer_load_dwordx4 v[252:255], v29, s[36:39], s92 offen offset:256 nt\n\t"
    "buffer_load_dwordx4 a[0:3], v29, s[36:39], s92 offen offset:512 nt\n\t"
    "buffer_load_dwordx4 a[4:7], v30, s[40:43], s92 offen nt\n\t"
    "buffer_load_dwordx4 a[8:11], v31, s[40:43], s92 offen nt\n\t"
    "buffer_load_dwordx4 a[12:15], v30, s[40:43], s92 offen offset:1664 nt\n\t"
    "v_mfma_f32_16x16x32_bf16 v[0:3], v[16:19], a[112:115], 0\n\t"
    "v_mfma_f32_16x16x32_bf16 v[0:3], v[20:23], a[116:119], v[0:3]\n\t"
    "v_mfma_f32_16x16x32_bf16 v[0:3], v[24:27], a[120:123], v[0:3]\n\t"
    "s_call_b64 s[58:59], .Lat_soft\n\t"
    "v_mfma_f32_16x16x32_bf16 v[4:7], v[0:3], a[124:127], v[4:7]\n\t"
    "v_mfma_f32_16x16x32_bf16 v[8:11], v[0:3], a[128:131], v[8:11]\n\t"
    "v_mfma_f32_16x16x32_bf16 v[12:15], v[0:3], a[132:135], v[12:15]\n\t"
    "v_mfma_f32_16x16x32_bf16 v[0:3], v[16:19], a[136:139], 0\n\t"
    "v_mfma_f32_16x16x32_bf16 v[0:3], v[20:23], a[140:143], v[0:3]\n\t"
    "v_mfma_f32_16x16x32_bf16 v[0:3], v[24:27], a[144:147], v[0:3]\n\t"
    "s_call_b64 s[58:59], .Lat_soft\n\t"
    "v_mfma_f32_16x16x32_bf16 v[4:7], v[0:3], a[148:151], v[4:7]\n\t"
    "v_mfma_f32_16x16x32_bf16 v[8:11], v[0:3], a[152:155], v[8:11]\n\t"
    "v_mfma_f32_16x16x32_bf16 v[12:15], v[0:3], a[156:159], v[12:15]\n\t"
    "v_mfma_f32_16x16x32_bf16 v[0:3], v[16:19], a[160:163], 0\n\t"
    "v_mfma_f32_16x16x32_bf16 v[0:3], v[20:23], a[164:167], v[0:3]\n\t"
    "v_mfma_f32_16x16x32_bf16 v[0:3], v[24:27], a[168:171], v[0:3]\n\t"
    "s_call_b64 s[58:59], .Lat_soft\n\t"
    "v_mfma_f32_16x16x32_bf16 v[4:7], v[0:3], a[172:175], v[4:7]\n\t"
    "v_mfma_f32_16x16x32_bf16 v[8:11], v[0:3], a[176:179], v[8:11]\n\t"
    "v_mfma_f32_16x16x32_bf16 v[12:15], v[0:3], a[180:183], v[12:15]\n\t"
    "s_add_u32 s92, s83, 688128\n\t"
    "s_cmp_gt_u32 s94, 8\n\t"
    "s_cselect_b32 s92, s92, 402653184\n\t"
    "buffer_load_dwordx4 a[16:19], v29, s[36:39], s92 offen offset:0 nt\n\t"
    "buffer_load_dwordx4 a[20:23], v29, s[36:39], s92 offen offset:256 nt\n\t"
    "buffer_load_dwordx4 a[24:27], v29, s[36:39], s92 offen offset:512 nt\n\t"
    "buffer_load_dwordx4 a[28:31], v30, s[40:43], s92 offen nt\n\t"
    "buffer_load_dwordx4 a[32:35], v31, s[40:43], s92 offen nt\n\t"
    "buffer_load_dwordx4 a[36:39], v30, s[40:43], s92 offen offset:1664 nt\n\t"
    "v_mfma_f32_16x16x32_bf16 v[0:3], v[16:19], a[184:187], 0\n\t"
    "v_mfma_f32_16x16x32_bf16 v[0:3], v[20:23], a[188:191], v[0:3]\n\t"
    "v_mfma_f32_16x16x32_bf16 v[0:3], v[24:27], a[192:195], v[0:3]\n\t"
    "s_call_b64 s[58:59], .Lat_soft\n\t"
    "v_mfma_f32_16x16x32_bf16 v[4:7], v[0:3], a[196:199], v[4:7]\n\t"
    "v_mfma_f32_16x16x32_bf16 v[8:11], v[0:3], a[200:203], v[8:11]\n\t"
    "v_mfma_f32_16x16x32_bf16 v[12:15], v[0:3], a[204:207], v[12:15]\n\t"
    "s_setpc_b64 s[80:81]\n\t"
    ".Lat_end:\n\t"
    :: "s"(Kc), "s"(Vc), "s"(ws), "s"(off0), "s"(nm), "s"(w), "s"(flg), "s"(part), "s"(qp), "s"(hf), "s"(dbg)
    : "memory", "v255", "a255", "s24", "s25", "s26", "s27", "s28", "s29", "s30", "s31", "s32", "s33", "s34", "s35", "s36", "s37", "s38", "s39", "s40", "s41", "s42", "s43", "s44", "s45", "s46", "s47", "s48", "s49", "s50", "s51", "s52", "s53", "s54", "s55", "s56", "s57", "s58", "s59", "s60", "s61", "s62", "s63", "s64", "s65", "s66", "s67", "s68", "s69", "s70", "s71", "s72", "s73", "s74", "s75", "s76", "s77", "s78", "s79", "s80", "s81", "s82", "s83", "s84", "s85", "s86", "s87", "s88", "s89", "s90", "s91", "s92", "s93", "s94", "s95", "s96", "s97", "s98", "s99");
    return;
  }
  const __amdgpu_buffer_rsrc_t rs = __builtin_amdgcn_make_buffer_rsrc((void*)ws, 0, WS_BYTES, 0x00020000);
  const u64 g1 = 0xFFFF0000ull, g2 = 0xFFFFull << 32, g3 = 0xFFFFull << 48;
  const unsigned stage = GU_OFF + 30720 * w;
  const int rho = h + 32 * w;
  const unsigned yw = Y_OFF + Y_STR * w, xs = Y_OFF + 2048 * w;
  constexpr float QS = 0.07216878364870322f * 1.4426950408889634f;
  STAMP(0);
  if (w == 3) {
    u64 pc; unsigned len;
    asm volatile("s_getpc_b64 %0\n.Lpf_here:\n\ts_mov_b32 %1, .Lat_end-fused_decode\n\ts_max_u32 %1, %1, .Lch_end-fused_decode" : "=s"(pc), "=s"(len) :: "scc");
    unsigned back;
    asm volatile("s_mov_b32 %0, .Lpf_here-fused_decode" : "=s"(back));
    const __amdgpu_buffer_rsrc_t rc = __builtin_amdgcn_make_buffer_rsrc((void*)(pc - back), 0, len, 0x00020000);
    const int lane = lane_id();
    unsigned acc = 0;
#pragma unroll
    for (int k = 0; k < 5; ++k) acc |= __builtin_amdgcn_raw_buffer_load_b32(rc, lane * 128, 8192 * k, 0);
    asm volatile("" :: "v"(acc));
  }
  const v4u srd_wo = mk_srd(wo, 6144 * 640 * 2), srd_wd = mk_srd(wd, 1536 * 640 * 2);
  const unsigned bw = h * 192 * 1280 + w * 10240;
  u64 ep;
  {
    LANE();
    const v4u z = {0u, 0u, 0u, 0u};
    { const int e = lane < 40 ? lane : 39;
#pragma unroll
      for (int k = 0; k < (TAB_G ? 4 : 2); ++k) __builtin_amdgcn_raw_buffer_store_b128(z, rs, P_OFF + h * 2560 + w * 640 + e * 16, 81920 * k, SC1); }
    __builtin_amdgcn_raw_buffer_store_b128(z, rs, Q_OFF + h * 4096 + (lane >> 3) * 512 + w * 128 + (lane & 7) * 16, 0, SC1);
    if (rho < 80 && lane < (TAB_G ? 6 : 4)) __builtin_amdgcn_raw_buffer_store_b128(z, rs, X_OFF + (lane >> 1) * 2560 + rho * 32 + (lane & 1) * 16, 0, SC1);
    unsigned xp[5], rp; v2u ev;
    { const unsigned zo = 0u; asm volatile("global_load_dwordx2 %0, %1, %2 sc1" : "=&v"(ev) : "v"(zo), "s"(ws) : "memory"); }
    { const unsigned xo = (w * 640 + 2 * lane) * 2, ro = (((lane >> 2) & 3) * 640 + 8 * (rho < 80 ? rho : 0) + 2 * (lane & 3)) * 2;
      asm volatile("global_load_dword %0, %6, %8\n\tglobal_load_dword %1, %6, %8 offset:256\n\tglobal_load_dword %2, %6, %8 offset:512\n\t"
                   "global_load_dword %3, %6, %8 offset:768\n\tglobal_load_dword %4, %6, %8 offset:1024\n\tglobal_load_dword %5, %7, %8"
                   : "=&v"(xp[0]), "=&v"(xp[1]), "=&v"(xp[2]), "=&v"(xp[3]), "=&v"(xp[4]), "=&v"(rp) : "v"(xo), "v"(ro), "s"(x0) : "memory"); }
    unsigned q0, q1, q2, so;
    { int G = lane; q0 = (G / 24) * 12288 + (G % 24) * 16; G += 64; q1 = (G / 24) * 12288 + (G % 24) * 16; G += 64; q2 = (G / 24) * 12288 + (G % 24) * 16; }
    const v4u sq = mk_srd(wq, 640 * 6144 * 2);
    const unsigned bq = h * 384 + w * 32 * 12288;
    asm volatile(
    "s_add_u32 %[so], %[bq], 0\n\t"
    "buffer_load_dwordx4 v[56:59], %[q0], %[sq], %[so] offen nt\n\t"
    "buffer_load_dwordx4 v[60:63], %[q1], %[sq], %[so] offen nt\n\t"
    "buffer_load_dwordx4 v[64:67], %[q2], %[sq], %[so] offen nt\n\t"
    "s_add_u32 %[so], %[bq], 98304\n\t"
    "buffer_load_dwordx4 v[68:71], %[q0], %[sq], %[so] offen nt\n\t"
    "buffer_load_dwordx4 v[72:75], %[q1], %[sq], %[so] offen nt\n\t"
    "buffer_load_dwordx4 v[76:79], %[q2], %[sq], %[so] offen nt\n\t"
    "s_add_u32 %[so], %[bq], 196608\n\t"
    "buffer_load_dwordx4 v[80:83], %[q0], %[sq], %[so] offen nt\n\t"
    "buffer_load_dwordx4 v[84:87], %[q1], %[sq], %[so] offen nt\n\t"
    "buffer_load_dwordx4 v[88:91], %[q2], %[sq], %[so] offen nt\n\t"
    "s_add_u32 %[so], %[bq], 294912\n\t"
    "buffer_load_dwordx4 v[92:95], %[q0], %[sq], %[so] offen nt\n\t"
    "buffer_load_dwordx4 v[96:99], %[q1], %[sq], %[so] offen nt\n\t"
    "buffer_load_dwordx4 v[100:103], %[q2], %[sq], %[so] offen nt\n\t"
    "s_add_u32 %[so], %[bq], 1572864\n\t"
    "buffer_load_dwordx4 v[104:107], %[q0], %[sq], %[so] offen nt\n\t"
    "buffer_load_dwordx4 v[108:111], %[q1], %[sq], %[so] offen nt\n\t"
    "buffer_load_dwordx4 v[112:115], %[q2], %[sq], %[so] offen nt\n\t"
    "s_add_u32 %[so], %[bq], 1671168\n\t"
    "buffer_load_dwordx4 v[116:119], %[q0], %[sq], %[so] offen nt\n\t"
    "buffer_load_dwordx4 v[120:123], %[q1], %[sq], %[so] offen nt\n\t"
    "buffer_load_dwordx4 v[124:127], %[q2], %[sq], %[so] offen nt\n\t"
    "s_add_u32 %[so], %[bq], 1769472\n\t"
    "buffer_load_dwordx4 v[128:131], %[q0], %[sq], %[so] offen nt\n\t"
    "buffer_load_dwordx4 v[132:135], %[q1], %[sq], %[so] offen nt\n\t"
    "buffer_load_dwordx4 v[136:139], %[q2], %[sq], %[so] offen nt\n\t"
    "s_add_u32 %[so], %[bq], 1867776\n\t"
    "buffer_load_dwordx4 v[140:143], %[q0], %[sq], %[so] offen nt\n\t"
    "buffer_load_dwordx4 v[144:147], %[q1], %[sq], %[so] offen nt\n\t"
    "buffer_load_dwordx4 v[148:151], %[q2], %[sq], %[so] offen nt\n\t"
      "s_waitcnt vmcnt(24)\n\ts_barrier"
      : [so] "=&s"(so), "+v"(xp[0]), "+v"(xp[1]), "+v"(xp[2]), "+v"(xp[3]), "+v"(xp[4]), "+v"(rp), "+v"(ev) : [q0] "v"(q0), [q1] "v"(q1), [q2] "v"(q2), [sq] "s"(sq), [bq] "s"(bq) : "memory", "scc");
    STAMP(3);
    ep = (((u64)(unsigned)__builtin_amdgcn_readfirstlane(ev.y) << 32) | (u64)(unsigned)__builtin_amdgcn_readfirstlane(ev.x)) + 1ull;
    if (w == 0 && lane == 0) __builtin_amdgcn_raw_buffer_store_b64(v2u{(unsigned)ep, (unsigned)(ep >> 32)}, rs, FLAGS_OFF + bid * 8, 0, SC1);
    float x[10];
#pragma unroll
    for (int i = 0; i < 5; ++i) { x[2 * i] = bf_lo(xp[i]); x[2 * i + 1] = bf_hi(xp[i]); }
    ln_to_lds(x, yw, lane);
    if (rho < 80 && lane < 16) {
      const unsigned ad = RES_OFF + ((lane >> 2) * 3 + w) * 32 + (lane & 3) * 8;
      lds_w32(ad, rp << 16); lds_w32(ad + 4, rp & 0xffff0000u);
    }
  }
  STAMP(4);
  {
    LANE();
    unsigned so;
    const unsigned ga = 16 * lane, wa = ga + 12288 * w, ia = WQI_OFF + 6144 * w + ga;
    const unsigned rq0 = (8 * c + (j >> 2)) * 384 + 8 * (j & 3) + 96 * w, rq1 = rq0 + 49152;
    unsigned q0, q1, q2;
    { int G = lane; q0 = (G / 24) * 12288 + (G % 24) * 16; G += 64; q1 = (G / 24) * 12288 + (G % 24) * 16; G += 64; q2 = (G / 24) * 12288 + (G % 24) * 16; }
    const v4u sq = mk_srd(wq, 640 * 6144 * 2);
    const unsigned bq = h * 384 + w * 32 * 12288;
    asm volatile(
    "s_waitcnt vmcnt(12)\n\t"
    "s_add_u32 %[so], %[bq], 3145728\n\t"
    "buffer_load_dwordx4 v[152:155], %[q0], %[sq], %[so] offen nt\n\t"
    "buffer_load_dwordx4 v[156:159], %[q1], %[sq], %[so] offen nt\n\t"
    "buffer_load_dwordx4 v[160:163], %[q2], %[sq], %[so] offen nt\n\t"
    "s_add_u32 %[so], %[bq], 3244032\n\t"
    "buffer_load_dwordx4 v[164:167], %[q0], %[sq], %[so] offen nt\n\t"
    "buffer_load_dwordx4 v[168:171], %[q1], %[sq], %[so] offen nt\n\t"
    "buffer_load_dwordx4 v[172:175], %[q2], %[sq], %[so] offen nt\n\t"
    "s_add_u32 %[so], %[bq], 3342336\n\t"
    "buffer_load_dwordx4 v[176:179], %[q0], %[sq], %[so] offen nt\n\t"
    "buffer_load_dwordx4 v[180:183], %[q1], %[sq], %[so] offen nt\n\t"
    "buffer_load_dwordx4 v[184:187], %[q2], %[sq], %[so] offen nt\n\t"
    "s_add_u32 %[so], %[bq], 3440640\n\t"
    "buffer_load_dwordx4 v[188:191], %[q0], %[sq], %[so] offen nt\n\t"
    "buffer_load_dwordx4 v[192:195], %[q1], %[sq], %[so] offen nt\n\t"
    "buffer_load_dwordx4 v[196:199], %[q2], %[sq], %[so] offen nt\n\t"
    "ds_write_b128 %[wa], v[56:59] offset:0\n\t"
    "ds_write_b128 %[wa], v[60:63] offset:1024\n\t"
    "ds_write_b128 %[wa], v[64:67] offset:2048\n\t"
    "ds_write_b128 %[wa], v[68:71] offset:3072\n\t"
    "ds_write_b128 %[wa], v[72:75] offset:4096\n\t"
    "ds_write_b128 %[wa], v[76:79] offset:5120\n\t"
    "ds_write_b128 %[wa], v[80:83] offset:6144\n\t"
    "ds_write_b128 %[wa], v[84:87] offset:7168\n\t"
    "ds_write_b128 %[wa], v[88:91] offset:8192\n\t"
    "ds_write_b128 %[wa], v[92:95] offset:9216\n\t"
    "ds_write_b128 %[wa], v[96:99] offset:10240\n\t"
    "ds_write_b128 %[wa], v[100:103] offset:11264\n\t"
    "s_waitcnt lgkmcnt(0)\n\t"
    "s_barrier\n\t"
    "ds_read_b64_tr_b16 v[56:57], %[rq0] offset:0\n\t"
    "ds_read_b64_tr_b16 v[58:59], %[rq0] offset:1536\n\t"
    "ds_read_b64_tr_b16 v[60:61], %[rq0] offset:32\n\t"
    "ds_read_b64_tr_b16 v[62:63], %[rq0] offset:1568\n\t"
    "ds_read_b64_tr_b16 v[64:65], %[rq0] offset:64\n\t"
    "ds_read_b64_tr_b16 v[66:67], %[rq0] offset:1600\n\t"
    "ds_read_b64_tr_b16 v[68:69], %[rq0] offset:12288\n\t"
    "ds_read_b64_tr_b16 v[70:71], %[rq0] offset:13824\n\t"
    "ds_read_b64_tr_b16 v[72:73], %[rq0] offset:12320\n\t"
    "ds_read_b64_tr_b16 v[74:75], %[rq0] offset:13856\n\t"
    "ds_read_b64_tr_b16 v[76:77], %[rq0] offset:12352\n\t"
    "ds_read_b64_tr_b16 v[78:79], %[rq0] offset:13888\n\t"
    "ds_read_b64_tr_b16 v[80:81], %[rq0] offset:24576\n\t"
    "ds_read_b64_tr_b16 v[82:83], %[rq0] offset:26112\n\t"
    "ds_read_b64_tr_b16 v[84:85], %[rq0] offset:24608\n\t"
    "ds_read_b64_tr_b16 v[86:87], %[rq0] offset:26144\n\t"
    "ds_read_b64_tr_b16 v[88:89], %[rq0] offset:24640\n\t"
    "ds_read_b64_tr_b16 v[90:91], %[rq0] offset:26176\n\t"
    "ds_read_b64_tr_b16 v[92:93], %[rq0] offset:36864\n\t"
    "ds_read_b64_tr_b16 v[94:95], %[rq0] offset:38400\n\t"
    "ds_read_b64_tr_b16 v[96:97], %[rq0] offset:36896\n\t"
    "ds_read_b64_tr_b16 v[98:99], %[rq0] offset:38432\n\t"
    "ds_read_b64_tr_b16 v[100:101], %[rq0] offset:36928\n\t"
    "ds_read_b64_tr_b16 v[102:103], %[rq0] offset:38464\n\t"
    "s_waitcnt vmcnt(12)\n\t"
    "s_add_u32 %[so], %[bq], 4718592\n\t"
    "buffer_load_dwordx4 v[200:203], %[q0], %[sq], %[so] offen nt\n\t"
    "buffer_load_dwordx4 v[204:207], %[q1], %[sq], %[so] offen nt\n\t"
    "buffer_load_dwordx4 v[208:211], %[q2], %[sq], %[so] offen nt\n\t"
    "s_add_u32 %[so], %[bq], 4816896\n\t"
    "buffer_load_dwordx4 v[212:215], %[q0], %[sq], %[so] offen nt\n\t"
    "buffer_load_dwordx4 v[216:219], %[q1], %[sq], %[so] offen nt\n\t"
    "buffer_load_dwordx4 v[220:223], %[q2], %[sq], %[so] offen nt\n\t"
    "s_add_u32 %[so], %[bq], 4915200\n\t"
    "buffer_load_dwordx4 v[224:227], %[q0], %[sq], %[so] offen nt\n\t"
    "buffer_load_dwordx4 v[228:231], %[q1], %[sq], %[so] offen nt\n\t"
    "buffer_load_dwordx4 v[232:235], %[q2], %[sq], %[so] offen nt\n\t"
    "s_add_u32 %[so], %[bq], 5013504\n\t"
    "buffer_load_dwordx4 v[236:239], %[q0], %[sq], %[so] offen nt\n\t"
    "buffer_load_dwordx4 v[240:243], %[q1], %[sq], %[so] offen nt\n\t"
    "buffer_load_dwordx4 v[244:247], %[q2], %[sq], %[so] offen nt\n\t"
    "ds_write_b128 %[wa], v[104:107] offset:49152\n\t"
    "ds_write_b128 %[wa], v[108:111] offset:50176\n\t"
    "ds_write_b128 %[wa], v[112:115] offset:51200\n\t"
    "ds_write_b128 %[wa], v[116:119] offset:52224\n\t"
    "ds_write_b128 %[wa], v[120:123] offset:53248\n\t"
    "ds_write_b128 %[wa], v[124:127] offset:54272\n\t"
    "ds_write_b128 %[wa], v[128:131] offset:55296\n\t"
    "ds_write_b128 %[wa], v[132:135] offset:56320\n\t"
    "ds_write_b128 %[wa], v[136:139] offset:57344\n\t"
    "ds_write_b128 %[wa], v[140:143] offset:58368\n\t"
    "ds_write_b128 %[wa], v[144:147] offset:59392\n\t"
    "ds_write_b128 %[wa], v[148:151] offset:60416\n\t"
    "s_waitcnt lgkmcnt(0)\n\t"
    "s_barrier\n\t"
    "ds_read_b64_tr_b16 v[104:105], %[rq1] offset:0\n\t"
    "ds_read_b64_tr_b16 v[106:107], %[rq1] offset:1536\n\t"
    "ds_read_b64_tr_b16 v[108:109], %[rq1] offset:32\n\t"
    "ds_read_b64_tr_b16 v[110:111], %[rq1] offset:1568\n\t"
    "ds_read_b64_tr_b16 v[112:113], %[rq1] offset:64\n\t"
    "ds_read_b64_tr_b16 v[114:115], %[rq1] offset:1600\n\t"
    "ds_read_b64_tr_b16 v[116:117], %[rq1] offset:12288\n\t"
    "ds_read_b64_tr_b16 v[118:119], %[rq1] offset:13824\n\t"
    "ds_read_b64_tr_b16 v[120:121], %[rq1] offset:12320\n\t"
    "ds_read_b64_tr_b16 v[122:123], %[rq1] offset:13856\n\t"
    "ds_read_b64_tr_b16 v[124:125], %[rq1] offset:12352\n\t"
    "ds_read_b64_tr_b16 v[126:127], %[rq1] offset:13888\n\t"
    "ds_read_b64_tr_b16 v[128:129], %[rq1] offset:24576\n\t"
    "ds_read_b64_tr_b16 v[130:131], %[rq1] offset:26112\n\t"
    "ds_read_b64_tr_b16 v[132:133], %[rq1] offset:24608\n\t"
    "ds_read_b64_tr_b16 v[134:135], %[rq1] offset:26144\n\t"
    "ds_read_b64_tr_b16 v[136:137], %[rq1] offset:24640\n\t"
    "ds_read_b64_tr_b16 v[138:139], %[rq1] offset:26176\n\t"
    "ds_read_b64_tr_b16 v[140:141], %[rq1] offset:36864\n\t"
    "ds_read_b64_tr_b16 v[142:143], %[rq1] offset:38400\n\t"
    "ds_read_b64_tr_b16 v[144:145], %[rq1] offset:36896\n\t"
    "ds_read_b64_tr_b16 v[146:147], %[rq1] offset:38432\n\t"
    "ds_read_b64_tr_b16 v[148:149], %[rq1] offset:36928\n\t"
    "ds_read_b64_tr_b16 v[150:151], %[rq1] offset:38464\n\t"
    "s_waitcnt vmcnt(12)\n\t"
    "s_add_u32 %[so], %[bq], 6291456\n\t"
    "buffer_load_dwordx4 v[248:251], %[q0], %[sq], %[so] offen nt\n\t"
    "buffer_load_dwordx4 v[252:255], %[q1], %[sq], %[so] offen nt\n\t"
    "buffer_load_dwordx4 a[0:3], %[q2], %[sq], %[so] offen nt\n\t"
    "s_add_u32 %[so], %[bq], 6389760\n\t"
    "buffer_load_dwordx4 a[4:7], %[q0], %[sq], %[so] offen nt\n\t"
    "buffer_load_dwordx4 a[8:11], %[q1], %[sq], %[so] offen nt\n\t"
    "buffer_load_dwordx4 a[12:15], %[q2], %[sq], %[so] offen nt\n\t"
    "s_add_u32 %[so], %[bq], 6488064\n\t"
    "buffer_load_dwordx4 a[16:19], %[q0], %[sq], %[so] offen nt\n\t"
    "buffer_load_dwordx4 a[20:23], %[q1], %[sq], %[so] offen nt\n\t"
    "buffer_load_dwordx4 a[24:27], %[q2], %[sq], %[so] offen nt\n\t"
    "s_add_u32 %[so], %[bq], 6586368\n\t"
    "buffer_load_dwordx4 a[28:31], %[q0], %[sq], %[so] offen nt\n\t"
    "buffer_load_dwordx4 a[32:35], %[q1], %[sq], %[so] offen nt\n\t"
    "buffer_load_dwordx4 a[36:39], %[q2], %[sq], %[so] offen nt\n\t"
    "ds_write_b128 %[wa], v[152:155] offset:0\n\t"
    "ds_write_b128 %[wa], v[156:159] offset:1024\n\t"
    "ds_write_b128 %[wa], v[160:163] offset:2048\n\t"
    "ds_write_b128 %[wa], v[164:167] offset:3072\n\t"
    "ds_write_b128 %[wa], v[168:171] offset:4096\n\t"
    "ds_write_b128 %[wa], v[172:175] offset:5120\n\t"
    "ds_write_b128 %[wa], v[176:179] offset:6144\n\t"
    "ds_write_b128 %[wa], v[180:183] offset:7168\n\t"
    "ds_write_b128 %[wa], v[184:187] offset:8192\n\t"
    "ds_write_b128 %[wa], v[188:191] offset:9216\n\t"
    "ds_write_b128 %[wa], v[192:195] offset:10240\n\t"
    "ds_write_b128 %[wa], v[196:199] offset:11264\n\t"
    "s_waitcnt lgkmcnt(0)\n\t"
    "s_barrier\n\t"
    "ds_read_b64_tr_b16 v[152:153], %[rq0] offset:0\n\t"
    "ds_read_b64_tr_b16 v[154:155], %[rq0] offset:1536\n\t"
    "ds_read_b64_tr_b16 v[156:157], %[rq0] offset:32\n\t"
    "ds_read_b64_tr_b16 v[158:159], %[rq0] offset:1568\n\t"
    "ds_read_b64_tr_b16 v[160:161], %[rq0] offset:64\n\t"
    "ds_read_b64_tr_b16 v[162:163], %[rq0] offset:1600\n\t"
    "ds_read_b64_tr_b16 v[164:165], %[rq0] offset:12288\n\t"
    "ds_read_b64_tr_b16 v[166:167], %[rq0] offset:13824\n\t"
    "ds_read_b64_tr_b16 v[168:169], %[rq0] offset:12320\n\t"
    "ds_read_b64_tr_b16 v[170:171], %[rq0] offset:13856\n\t"
    "ds_read_b64_tr_b16 v[172:173], %[rq0] offset:12352\n\t"
    "ds_read_b64_tr_b16 v[174:175], %[rq0] offset:13888\n\t"
    "ds_read_b64_tr_b16 v[176:177], %[rq0] offset:24576\n\t"
    "ds_read_b64_tr_b16 v[178:179], %[rq0] offset:26112\n\t"
    "ds_read_b64_tr_b16 v[180:181], %[rq0] offset:24608\n\t"
    "ds_read_b64_tr_b16 v[182:183], %[rq0] offset:26144\n\t"
    "ds_read_b64_tr_b16 v[184:185], %[rq0] offset:24640\n\t"
    "ds_read_b64_tr_b16 v[186:187], %[rq0] offset:26176\n\t"
    "ds_read_b64_tr_b16 v[188:189], %[rq0] offset:36864\n\t"
    "ds_read_b64_tr_b16 v[190:191], %[rq0] offset:38400\n\t"
    "ds_read_b64_tr_b16 v[192:193], %[rq0] offset:36896\n\t"
    "ds_read_b64_tr_b16 v[194:195], %[rq0] offset:38432\n\t"
    "ds_read_b64_tr_b16 v[196:197], %[rq0] offset:36928\n\t"
    "ds_read_b64_tr_b16 v[198:199], %[rq0] offset:38464\n\t"
    "s_waitcnt vmcnt(12)\n\t"
    "s_add_u32 %[so], %[bw], 40960\n\t"
    "buffer_load_dwordx4 a[204:207], %[ga], %[sw], %[so] offen offset:0 nt\n\t"
    "buffer_load_dwordx4 a[208:211], %[ga], %[sw], %[so] offen offset:1024 nt\n\t"
    "buffer_load_dwordx4 a[248:251], %[ga], %[sw], %[so] offen offset:2048 nt\n\t"
    "buffer_load_dwordx4 a[140:143], %[ga], %[sw], %[so] offen offset:3072 nt\n\t"
    "s_add_u32 %[so], %[bw], 45056\n\t"
    "buffer_load_dwordx4 a[144:147], %[ga], %[sw], %[so] offen offset:0 nt\n\t"
    "buffer_load_dwordx4 a[148:151], %[ga], %[sw], %[so] offen offset:1024 nt\n\t"
    "buffer_load_dwordx4 a[152:155], %[ga], %[sw], %[so] offen offset:2048 nt\n\t"
    "buffer_load_dwordx4 a[156:159], %[ga], %[sw], %[so] offen offset:3072 nt\n\t"
    "s_add_u32 %[so], %[bw], 49152\n\t"
    "buffer_load_dwordx4 a[160:163], %[ga], %[sw], %[so] offen offset:0 nt\n\t"
    "buffer_load_dwordx4 a[164:167], %[ga], %[sw], %[so] offen offset:1024 nt\n\t"
    "ds_write_b128 %[wa], v[200:203] offset:49152\n\t"
    "ds_write_b128 %[wa], v[204:207] offset:50176\n\t"
    "ds_write_b128 %[wa], v[208:211] offset:51200\n\t"
    "ds_write_b128 %[wa], v[212:215] offset:52224\n\t"
    "ds_write_b128 %[wa], v[216:219] offset:53248\n\t"
    "ds_write_b128 %[wa], v[220:223] offset:54272\n\t"
    "ds_write_b128 %[wa], v[224:227] offset:55296\n\t"
    "ds_write_b128 %[wa], v[228:231] offset:56320\n\t"
    "ds_write_b128 %[wa], v[232:235] offset:57344\n\t"
    "ds_write_b128 %[wa], v[236:239] offset:58368\n\t"
    "ds_write_b128 %[wa], v[240:243] offset:59392\n\t"
    "ds_write_b128 %[wa], v[244:247] offset:60416\n\t"
    "s_waitcnt lgkmcnt(0)\n\t"
    "s_barrier\n\t"
    "ds_read_b64_tr_b16 v[200:201], %[rq1] offset:0\n\t"
    "ds_read_b64_tr_b16 v[202:203], %[rq1] offset:1536\n\t"
    "ds_read_b64_tr_b16 v[204:205], %[rq1] offset:32\n\t"
    "ds_read_b64_tr_b16 v[206:207], %[rq1] offset:1568\n\t"
    "ds_read_b64_tr_b16 v[208:209], %[rq1] offset:64\n\t"
    "ds_read_b64_tr_b16 v[210:211], %[rq1] offset:1600\n\t"
    "ds_read_b64_tr_b16 v[212:213], %[rq1] offset:12288\n\t"
    "ds_read_b64_tr_b16 v[214:215], %[rq1] offset:13824\n\t"
    "ds_read_b64_tr_b16 v[216:217], %[rq1] offset:12320\n\t"
    "ds_read_b64_tr_b16 v[218:219], %[rq1] offset:13856\n\t"
    "ds_read_b64_tr_b16 v[220:221], %[rq1] offset:12352\n\t"
    "ds_read_b64_tr_b16 v[222:223], %[rq1] offset:13888\n\t"
    "ds_read_b64_tr_b16 v[224:225], %[rq1] offset:24576\n\t"
    "ds_read_b64_tr_b16 v[226:227], %[rq1] offset:26112\n\t"
    "ds_read_b64_tr_b16 v[228:229], %[rq1] offset:24608\n\t"
    "ds_read_b64_tr_b16 v[230:231], %[rq1] offset:26144\n\t"
    "ds_read_b64_tr_b16 v[232:233], %[rq1] offset:24640\n\t"
    "ds_read_b64_tr_b16 v[234:235], %[rq1] offset:26176\n\t"
    "ds_read_b64_tr_b16 v[236:237], %[rq1] offset:36864\n\t"
    "ds_read_b64_tr_b16 v[238:239], %[rq1] offset:38400\n\t"
    "ds_read_b64_tr_b16 v[240:241], %[rq1] offset:36896\n\t"
    "ds_read_b64_tr_b16 v[242:243], %[rq1] offset:38432\n\t"
    "ds_read_b64_tr_b16 v[244:245], %[rq1] offset:36928\n\t"
    "ds_read_b64_tr_b16 v[246:247], %[rq1] offset:38464\n\t"
    "s_waitcnt vmcnt(10)\n\t"
    "s_add_u32 %[so], %[bw], 81920\n\t"
    "buffer_load_dwordx4 a[212:215], %[ga], %[sw], %[so] offen offset:0 nt\n\t"
    "buffer_load_dwordx4 a[216:219], %[ga], %[sw], %[so] offen offset:1024 nt\n\t"
    "buffer_load_dwordx4 a[252:255], %[ga], %[sw], %[so] offen offset:2048 nt\n\t"
    "buffer_load_dwordx4 a[112:115], %[ga], %[sw], %[so] offen offset:3072 nt\n\t"
    "s_add_u32 %[so], %[bw], 86016\n\t"
    "buffer_load_dwordx4 a[116:119], %[ga], %[sw], %[so] offen offset:0 nt\n\t"
    "buffer_load_dwordx4 a[120:123], %[ga], %[sw], %[so] offen offset:1024 nt\n\t"
    "buffer_load_dwordx4 a[124:127], %[ga], %[sw], %[so] offen offset:2048 nt\n\t"
    "buffer_load_dwordx4 a[128:131], %[ga], %[sw], %[so] offen offset:3072 nt\n\t"
    "s_add_u32 %[so], %[bw], 90112\n\t"
    "buffer_load_dwordx4 a[132:135], %[ga], %[sw], %[so] offen offset:0 nt\n\t"
    "buffer_load_dwordx4 a[136:139], %[ga], %[sw], %[so] offen offset:1024 nt\n\t"
    "ds_write_b128 %[wa], v[248:251] offset:0\n\t"
    "ds_write_b128 %[wa], v[252:255] offset:1024\n\t"
    "ds_write_b128 %[wa], a[0:3] offset:2048\n\t"
    "ds_write_b128 %[wa], a[4:7] offset:3072\n\t"
    "ds_write_b128 %[wa], a[8:11] offset:4096\n\t"
    "ds_write_b128 %[wa], a[12:15] offset:5120\n\t"
    "ds_write_b128 %[wa], a[16:19] offset:6144\n\t"
    "ds_write_b128 %[wa], a[20:23] offset:7168\n\t"
    "ds_write_b128 %[wa], a[24:27] offset:8192\n\t"
    "ds_write_b128 %[wa], a[28:31] offset:9216\n\t"
    "ds_write_b128 %[wa], a[32:35] offset:10240\n\t"
    "ds_write_b128 %[wa], a[36:39] offset:11264\n\t"
    "s_waitcnt lgkmcnt(0)\n\t"
    "s_barrier\n\t"
    "ds_read_b64_tr_b16 v[248:249], %[rq0] offset:0\n\t"
    "ds_read_b64_tr_b16 v[250:251], %[rq0] offset:1536\n\t"
    "ds_read_b64_tr_b16 v[252:253], %[rq0] offset:32\n\t"
    "ds_read_b64_tr_b16 v[254:255], %[rq0] offset:1568\n\t"
    "ds_read_b64_tr_b16 a[0:1], %[rq0] offset:64\n\t"
    "ds_read_b64_tr_b16 a[2:3], %[rq0] offset:1600\n\t"
    "ds_read_b64_tr_b16 a[4:5], %[rq0] offset:12288\n\t"
    "ds_read_b64_tr_b16 a[6:7], %[rq0] offset:13824\n\t"
    "ds_read_b64_tr_b16 a[8:9], %[rq0] offset:12320\n\t"
    "ds_read_b64_tr_b16 a[10:11], %[rq0] offset:13856\n\t"
    "ds_read_b64_tr_b16 a[12:13], %[rq0] offset:12352\n\t"
    "ds_read_b64_tr_b16 a[14:15], %[rq0] offset:13888\n\t"
    "ds_read_b64_tr_b16 a[16:17], %[rq0] offset:24576\n\t"
    "ds_read_b64_tr_b16 a[18:19], %[rq0] offset:26112\n\t"
    "ds_read_b64_tr_b16 a[20:21], %[rq0] offset:24608\n\t"
    "ds_read_b64_tr_b16 a[22:23], %[rq0] offset:26144\n\t"
    "ds_read_b64_tr_b16 a[24:25], %[rq0] offset:24640\n\t"
    "ds_read_b64_tr_b16 a[26:27], %[rq0] offset:26176\n\t"
    "ds_read_b64_tr_b16 a[28:29], %[rq0] offset:36864\n\t"
    "ds_read_b64_tr_b16 a[30:31], %[rq0] offset:38400\n\t"
    "ds_read_b64_tr_b16 a[32:33], %[rq0] offset:36896\n\t"
    "ds_read_b64_tr_b16 a[34:35], %[rq0] offset:38432\n\t"
    "ds_read_b64_tr_b16 a[36:37], %[rq0] offset:36928\n\t"
    "ds_read_b64_tr_b16 a[38:39], %[rq0] offset:38464\n\t"
    "s_waitcnt lgkmcnt(0)\n\t"
    "ds_write_b128 %[ia], a[16:19] offset:0\n\t"
    "ds_write_b128 %[ia], a[20:23] offset:1024\n\t"
    "ds_write_b128 %[ia], a[24:27] offset:2048\n\t"
    "ds_write_b128 %[ia], a[28:31] offset:3072\n\t"
    "ds_write_b128 %[ia], a[32:35] offset:4096\n\t"
    "ds_write_b128 %[ia], a[36:39] offset:5120\n\t"
    "s_waitcnt lgkmcnt(0)\n\t"
      : [so] "=&s"(so) : [ga] "v"(ga), [wa] "v"(wa), [rq0] "v"(rq0), [rq1] "v"(rq1), [ia] "v"(ia), [sw] "s"(srd_wo), [bw] "s"(bw), [q0] "v"(q0), [q1] "v"(q1), [q2] "v"(q2), [sq] "s"(sq), [bq] "s"(bq) : "memory", "scc");
  }
  STAMP(5);
  BAR();
  {
    LANE();
    float o0, o1, o2;
    const unsigned ya = Y_OFF + Y_STR * (j >> 2) + Y_LO * (j & 1) + 16 * c, ia = WQI_OFF + 6144 * w + 16 * lane;
    asm volatile(
    "ds_read_b128 v[16:19], %[ia] offset:0\n\t"
    "ds_read_b128 v[20:23], %[ia] offset:1024\n\t"
    "ds_read_b128 v[24:27], %[ia] offset:2048\n\t"
    "ds_read_b128 v[28:31], %[ya] offset:1152\n\t"
    "ds_read_b128 v[32:35], %[ya] offset:0\n\t"
    "ds_read_b128 v[36:39], %[ya] offset:64\n\t"
    "s_waitcnt lgkmcnt(2)\n\t"
    "v_mfma_f32_16x16x32_bf16 v[40:43], v[28:31], v[16:19], 0\n\t"
    "v_mfma_f32_16x16x32_bf16 v[44:47], v[28:31], v[20:23], 0\n\t"
    "v_mfma_f32_16x16x32_bf16 v[48:51], v[28:31], v[24:27], 0\n\t"
    "ds_read_b128 v[16:19], %[ia] offset:3072\n\t"
    "ds_read_b128 v[20:23], %[ia] offset:4096\n\t"
    "ds_read_b128 v[24:27], %[ia] offset:5120\n\t"
    "ds_read_b128 v[28:31], %[ya] offset:1216\n\t"
    "s_waitcnt lgkmcnt(5)\n\t"
    "v_mfma_f32_16x16x32_bf16 v[40:43], v[32:35], v[56:59], v[40:43]\n\t"
    "v_mfma_f32_16x16x32_bf16 v[44:47], v[32:35], v[60:63], v[44:47]\n\t"
    "v_mfma_f32_16x16x32_bf16 v[48:51], v[32:35], v[64:67], v[48:51]\n\t"
    "s_waitcnt lgkmcnt(4)\n\t"
    "v_mfma_f32_16x16x32_bf16 v[40:43], v[36:39], v[68:71], v[40:43]\n\t"
    "v_mfma_f32_16x16x32_bf16 v[44:47], v[36:39], v[72:75], v[44:47]\n\t"
    "v_mfma_f32_16x16x32_bf16 v[48:51], v[36:39], v[76:79], v[48:51]\n\t"
    "ds_read_b128 v[32:35], %[ya] offset:128\n\t"
    "ds_read_b128 v[36:39], %[ya] offset:192\n\t"
    "s_waitcnt lgkmcnt(2)\n\t"
    "v_mfma_f32_16x16x32_bf16 v[40:43], v[28:31], v[16:19], v[40:43]\n\t"
    "v_mfma_f32_16x16x32_bf16 v[44:47], v[28:31], v[20:23], v[44:47]\n\t"
    "v_mfma_f32_16x16x32_bf16 v[48:51], v[28:31], v[24:27], v[48:51]\n\t"
    "ds_read_b128 v[16:19], %[ya] offset:256\n\t"
    "ds_read_b128 v[20:23], %[ya] offset:320\n\t"
    "ds_read_b128 v[24:27], %[ya] offset:384\n\t"
    "ds_read_b128 v[28:31], %[ya] offset:448\n\t"
    "s_waitcnt lgkmcnt(5)\n\t"
    "v_mfma_f32_16x16x32_bf16 v[40:43], v[32:35], v[80:83], v[40:43]\n\t"
    "v_mfma_f32_16x16x32_bf16 v[44:47], v[32:35], v[84:87], v[44:47]\n\t"
    "v_mfma_f32_16x16x32_bf16 v[48:51], v[32:35], v[88:91], v[48:51]\n\t"
    "ds_read_b128 v[32:35], %[ya] offset:512\n\t"
    "s_waitcnt lgkmcnt(5)\n\t"
    "v_mfma_f32_16x16x32_bf16 v[40:43], v[36:39], v[92:95], v[40:43]\n\t"
    "v_mfma_f32_16x16x32_bf16 v[44:47], v[36:39], v[96:99], v[44:47]\n\t"
    "v_mfma_f32_16x16x32_bf16 v[48:51], v[36:39], v[100:103], v[48:51]\n\t"
    "ds_read_b128 v[36:39], %[ya] offset:576\n\t"
    "s_waitcnt lgkmcnt(5)\n\t"
    "v_mfma_f32_16x16x32_bf16 v[40:43], v[16:19], v[104:107], v[40:43]\n\t"
    "v_mfma_f32_16x16x32_bf16 v[44:47], v[16:19], v[108:111], v[44:47]\n\t"
    "v_mfma_f32_16x16x32_bf16 v[48:51], v[16:19], v[112:115], v[48:51]\n\t"
    "ds_read_b128 v[16:19], %[ya] offset:640\n\t"
    "s_waitcnt lgkmcnt(5)\n\t"
    "v_mfma_f32_16x16x32_bf16 v[40:43], v[20:23], v[116:119], v[40:43]\n\t"
    "v_mfma_f32_16x16x32_bf16 v[44:47], v[20:23], v[120:123], v[44:47]\n\t"
    "v_mfma_f32_16x16x32_bf16 v[48:51], v[20:23], v[124:127], v[48:51]\n\t"
    "ds_read_b128 v[20:23], %[ya] offset:704\n\t"
    "s_waitcnt lgkmcnt(5)\n\t"
    "v_mfma_f32_16x16x32_bf16 v[40:43], v[24:27], v[128:131], v[40:43]\n\t"
    "v_mfma_f32_16x16x32_bf16 v[44:47], v[24:27], v[132:135], v[44:47]\n\t"
    "v_mfma_f32_16x16x32_bf16 v[48:51], v[24:27], v[136:139], v[48:51]\n\t"
    "ds_read_b128 v[24:27], %[ya] offset:768\n\t"
    "s_waitcnt lgkmcnt(5)\n\t"
    "v_mfma_f32_16x16x32_bf16 v[40:43], v[28:31], v[140:143], v[40:43]\n\t"
    "v_mfma_f32_16x16x32_bf16 v[44:47], v[28:31], v[144:147], v[44:47]\n\t"
    "v_mfma_f32_16x16x32_bf16 v[48:51], v[28:31], v[148:151], v[48:51]\n\t"
    "ds_read_b128 v[28:31], %[ya] offset:832\n\t"
    "s_waitcnt lgkmcnt(5)\n\t"
    "v_mfma_f32_16x16x32_bf16 v[40:43], v[32:35], v[152:155], v[40:43]\n\t"
    "v_mfma_f32_16x16x32_bf16 v[44:47], v[32:35], v[156:159], v[44:47]\n\t"
    "v_mfma_f32_16x16x32_bf16 v[48:51], v[32:35], v[160:163], v[48:51]\n\t"
    "ds_read_b128 v[32:35], %[ya] offset:896\n\t"
    "s_waitcnt lgkmcnt(5)\n\t"
    "v_mfma_f32_16x16x32_bf16 v[40:43], v[36:39], v[164:167], v[40:43]\n\t"
    "v_mfma_f32_16x16x32_bf16 v[44:47], v[36:39], v[168:171], v[44:47]\n\t"
    "v_mfma_f32_16x16x32_bf16 v[48:51], v[36:39], v[172:175], v[48:51]\n\t"
    "ds_read_b128 v[36:39], %[ya] offset:960\n\t"
    "s_waitcnt lgkmcnt(5)\n\t"
    "v_mfma_f32_16x16x32_bf16 v[40:43], v[16:19], v[176:179], v[40:43]\n\t"
    "v_mfma_f32_16x16x32_bf16 v[44:47], v[16:19], v[180:183], v[44:47]\n\t"
    "v_mfma_f32_16x16x32_bf16 v[48:51], v[16:19], v[184:187], v[48:51]\n\t"
    "ds_read_b128 v[16:19], %[ya] offset:1024\n\t"
    "s_waitcnt lgkmcnt(5)\n\t"
    "v_mfma_f32_16x16x32_bf16 v[40:43], v[20:23], v[188:191], v[40:43]\n\t"
    "v_mfma_f32_16x16x32_bf16 v[44:47], v[20:23], v[192:195], v[44:47]\n\t"
    "v_mfma_f32_16x16x32_bf16 v[48:51], v[20:23], v[196:199], v[48:51]\n\t"
    "ds_read_b128 v[20:23], %[ya] offset:1088\n\t"
    "s_waitcnt lgkmcnt(5)\n\t"
    "v_mfma_f32_16x16x32_bf16 v[40:43], v[24:27], v[200:203], v[40:43]\n\t"
    "v_mfma_f32_16x16x32_bf16 v[44:47], v[24:27], v[204:207], v[44:47]\n\t"
    "v_mfma_f32_16x16x32_bf16 v[48:51], v[24:27], v[208:211], v[48:51]\n\t"
    "s_waitcnt lgkmcnt(4)\n\t"
    "v_mfma_f32_16x16x32_bf16 v[40:43], v[28:31], v[212:215], v[40:43]\n\t"
    "v_mfma_f32_16x16x32_bf16 v[44:47], v[28:31], v[216:219], v[44:47]\n\t"
    "v_mfma_f32_16x16x32_bf16 v[48:51], v[28:31], v[220:223], v[48:51]\n\t"
    "s_waitcnt lgkmcnt(3)\n\t"
    "v_mfma_f32_16x16x32_bf16 v[40:43], v[32:35], v[224:227], v[40:43]\n\t"
    "v_mfma_f32_16x16x32_bf16 v[44:47], v[32:35], v[228:231], v[44:47]\n\t"
    "v_mfma_f32_16x16x32_bf16 v[48:51], v[32:35], v[232:235], v[48:51]\n\t"
    "s_waitcnt lgkmcnt(2)\n\t"
    "v_mfma_f32_16x16x32_bf16 v[40:43], v[36:39], v[236:239], v[40:43]\n\t"
    "v_mfma_f32_16x16x32_bf16 v[44:47], v[36:39], v[240:243], v[44:47]\n\t"
    "v_mfma_f32_16x16x32_bf16 v[48:51], v[36:39], v[244:247], v[48:51]\n\t"
    "s_waitcnt lgkmcnt(1)\n\t"
    "v_mfma_f32_16x16x32_bf16 v[40:43], v[16:19], v[248:251], v[40:43]\n\t"
    "v_mfma_f32_16x16x32_bf16 v[44:47], v[16:19], v[252:255], v[44:47]\n\t"
    "v_mfma_f32_16x16x32_bf16 v[48:51], v[16:19], a[0:3], v[48:51]\n\t"
    "s_waitcnt lgkmcnt(0)\n\t"
    "v_mfma_f32_16x16x32_bf16 v[40:43], v[20:23], a[4:7], v[40:43]\n\t"
    "v_mfma_f32_16x16x32_bf16 v[44:47], v[20:23], a[8:11], v[44:47]\n\t"
    "v_mfma_f32_16x16x32_bf16 v[48:51], v[20:23], a[12:15], v[48:51]\n\t"
    "s_nop 7\n\t"
    "v_add_f32 %[o0], v40, v41\n\t"
    "v_add_f32 %[o1], v44, v45\n\t"
    "v_add_f32 %[o2], v48, v49\n\t"
      : [o0] "=&v"(o0), [o1] "=&v"(o1), [o2] "=&v"(o2) : [ya] "v"(ya), [ia] "v"(ia) : "memory", TCLOB);
    __builtin_amdgcn_raw_buffer_store_b64(v2u{pk_bf16(o0 * QS, o1 * QS), (pk_bf16(o2 * QS, 0.f) & 0xffffu) | 0x10000u}, rs, Q_OFF + h * 4096 + c * 512 + w * 128 + j * 8, 0, SC1);
  }
  STAMP(1);
  {
    LANE();
    unsigned so, g[3], l[3];
    const unsigned ga = 16 * lane, wa10 = ga + 10240 * w, wa10b = wa10 + 61440;
    const unsigned r32 = (8 * c + (j >> 2)) * 1280 + 8 * (j & 3) + 320 * w, r32b = r32 + 61440;
#pragma unroll
    for (int p = 0; p < 3; ++p) { const int G = 64 * p + lane, row = G / 6, ch = G % 6; g[p] = row * 3072 + ch * 16;
      l[p] = stage + (ch >> 1) * 1024 + (row ^ (((row >> 3) & 1) << 2)) * 32 + (ch & 1) * 16; }
    const v4u sg = mk_srd(wg, 640 * 1536 * 2), su = mk_srd(wu, 640 * 1536 * 2);
    const unsigned bg = 160 * w * 3072 + h * 96;
    asm volatile(
    "s_waitcnt vmcnt(10)\n\t"
    "s_add_u32 %[so], %[bw], 0\n\t"
    "buffer_load_dwordx4 a[196:199], %[ga], %[sw], %[so] offen offset:0 nt\n\t"
    "buffer_load_dwordx4 a[200:203], %[ga], %[sw], %[so] offen offset:1024 nt\n\t"
    "buffer_load_dwordx4 a[244:247], %[ga], %[sw], %[so] offen offset:2048 nt\n\t"
    "buffer_load_dwordx4 a[168:171], %[ga], %[sw], %[so] offen offset:3072 nt\n\t"
    "s_add_u32 %[so], %[bw], 4096\n\t"
    "buffer_load_dwordx4 a[172:175], %[ga], %[sw], %[so] offen offset:0 nt\n\t"
    "buffer_load_dwordx4 a[176:179], %[ga], %[sw], %[so] offen offset:1024 nt\n\t"
    "buffer_load_dwordx4 a[180:183], %[ga], %[sw], %[so] offen offset:2048 nt\n\t"
    "buffer_load_dwordx4 a[184:187], %[ga], %[sw], %[so] offen offset:3072 nt\n\t"
    "s_add_u32 %[so], %[bw], 8192\n\t"
    "buffer_load_dwordx4 a[188:191], %[ga], %[sw], %[so] offen offset:0 nt\n\t"
    "buffer_load_dwordx4 a[192:195], %[ga], %[sw], %[so] offen offset:1024 nt\n\t"
    "ds_write_b128 %[wa10], a[204:207] offset:0\n\t"
    "ds_write_b128 %[wa10], a[208:211] offset:1024\n\t"
    "ds_write_b128 %[wa10], a[248:251] offset:2048\n\t"
    "ds_write_b128 %[wa10], a[140:143] offset:3072\n\t"
    "ds_write_b128 %[wa10], a[144:147] offset:4096\n\t"
    "ds_write_b128 %[wa10], a[148:151] offset:5120\n\t"
    "ds_write_b128 %[wa10], a[152:155] offset:6144\n\t"
    "ds_write_b128 %[wa10], a[156:159] offset:7168\n\t"
    "ds_write_b128 %[wa10], a[160:163] offset:8192\n\t"
    "ds_write_b128 %[wa10], a[164:167] offset:9216\n\t"
    "s_waitcnt lgkmcnt(0)\n\t"
    "s_barrier\n\t"
    "ds_read_b64_tr_b16 a[204:205], %[r32] offset:0\n\t"
    "ds_read_b64_tr_b16 a[206:207], %[r32] offset:5120\n\t"
    "ds_read_b64_tr_b16 a[208:209], %[r32] offset:32\n\t"
    "ds_read_b64_tr_b16 a[210:211], %[r32] offset:5152\n\t"
    "ds_read_b64_tr_b16 a[248:249], %[r32] offset:64\n\t"
    "ds_read_b64_tr_b16 a[250:251], %[r32] offset:5184\n\t"
    "ds_read_b64_tr_b16 a[140:141], %[r32] offset:96\n\t"
    "ds_read_b64_tr_b16 a[142:143], %[r32] offset:5216\n\t"
    "ds_read_b64_tr_b16 a[144:145], %[r32] offset:128\n\t"
    "ds_read_b64_tr_b16 a[146:147], %[r32] offset:5248\n\t"
    "ds_read_b64_tr_b16 a[148:149], %[r32] offset:160\n\t"
    "ds_read_b64_tr_b16 a[150:151], %[r32] offset:5280\n\t"
    "ds_read_b64_tr_b16 a[152:153], %[r32] offset:192\n\t"
    "ds_read_b64_tr_b16 a[154:155], %[r32] offset:5312\n\t"
    "ds_read_b64_tr_b16 a[156:157], %[r32] offset:224\n\t"
    "ds_read_b64_tr_b16 a[158:159], %[r32] offset:5344\n\t"
    "ds_read_b64_tr_b16 a[160:161], %[r32] offset:256\n\t"
    "ds_read_b64_tr_b16 a[162:163], %[r32] offset:5376\n\t"
    "ds_read_b64_tr_b16 a[164:165], %[r32] offset:288\n\t"
    "ds_read_b64_tr_b16 a[166:167], %[r32] offset:5408\n\t"
    "s_waitcnt vmcnt(10)\n\t"
    "s_add_u32 %[so], %[bw], 122880\n\t"
    "buffer_load_dwordx4 a[220:223], %[ga], %[sw], %[so] offen offset:0 nt\n\t"
    "buffer_load_dwordx4 a[224:227], %[ga], %[sw], %[so] offen offset:1024 nt\n\t"
    "buffer_load_dwordx4 a[80:83], %[ga], %[sw], %[so] offen offset:2048 nt\n\t"
    "buffer_load_dwordx4 a[84:87], %[ga], %[sw], %[so] offen offset:3072 nt\n\t"
    "s_add_u32 %[so], %[bw], 126976\n\t"
    "buffer_load_dwordx4 a[88:91], %[ga], %[sw], %[so] offen offset:0 nt\n\t"
    "buffer_load_dwordx4 a[92:95], %[ga], %[sw], %[so] offen offset:1024 nt\n\t"
    "buffer_load_dwordx4 a[96:99], %[ga], %[sw], %[so] offen offset:2048 nt\n\t"
    "buffer_load_dwordx4 a[100:103], %[ga], %[sw], %[so] offen offset:3072 nt\n\t"
    "s_add_u32 %[so], %[bw], 131072\n\t"
    "buffer_load_dwordx4 a[104:107], %[ga], %[sw], %[so] offen offset:0 nt\n\t"
    "buffer_load_dwordx4 a[108:111], %[ga], %[sw], %[so] offen offset:1024 nt\n\t"
    "ds_write_b128 %[wa10b], a[212:215] offset:0\n\t"
    "ds_write_b128 %[wa10b], a[216:219] offset:1024\n\t"
    "ds_write_b128 %[wa10b], a[252:255] offset:2048\n\t"
    "ds_write_b128 %[wa10b], a[112:115] offset:3072\n\t"
    "ds_write_b128 %[wa10b], a[116:119] offset:4096\n\t"
    "ds_write_b128 %[wa10b], a[120:123] offset:5120\n\t"
    "ds_write_b128 %[wa10b], a[124:127] offset:6144\n\t"
    "ds_write_b128 %[wa10b], a[128:131] offset:7168\n\t"
    "ds_write_b128 %[wa10b], a[132:135] offset:8192\n\t"
    "ds_write_b128 %[wa10b], a[136:139] offset:9216\n\t"
    "s_waitcnt lgkmcnt(0)\n\t"
    "s_barrier\n\t"
    "ds_read_b64_tr_b16 a[212:213], %[r32b] offset:0\n\t"
    "ds_read_b64_tr_b16 a[214:215], %[r32b] offset:5120\n\t"
    "ds_read_b64_tr_b16 a[216:217], %[r32b] offset:32\n\t"
    "ds_read_b64_tr_b16 a[218:219], %[r32b] offset:5152\n\t"
    "ds_read_b64_tr_b16 a[252:253], %[r32b] offset:64\n\t"
    "ds_read_b64_tr_b16 a[254:255], %[r32b] offset:5184\n\t"
    "ds_read_b64_tr_b16 a[112:113], %[r32b] offset:96\n\t"
    "ds_read_b64_tr_b16 a[114:115], %[r32b] offset:5216\n\t"
    "ds_read_b64_tr_b16 a[116:117], %[r32b] offset:128\n\t"
    "ds_read_b64_tr_b16 a[118:119], %[r32b] offset:5248\n\t"
    "ds_read_b64_tr_b16 a[120:121], %[r32b] offset:160\n\t"
    "ds_read_b64_tr_b16 a[122:123], %[r32b] offset:5280\n\t"
    "ds_read_b64_tr_b16 a[124:125], %[r32b] offset:192\n\t"
    "ds_read_b64_tr_b16 a[126:127], %[r32b] offset:5312\n\t"
    "ds_read_b64_tr_b16 a[128:129], %[r32b] offset:224\n\t"
    "ds_read_b64_tr_b16 a[130:131], %[r32b] offset:5344\n\t"
    "ds_read_b64_tr_b16 a[132:133], %[r32b] offset:256\n\t"
    "ds_read_b64_tr_b16 a[134:135], %[r32b] offset:5376\n\t"
    "ds_read_b64_tr_b16 a[136:137], %[r32b] offset:288\n\t"
    "ds_read_b64_tr_b16 a[138:139], %[r32b] offset:5408\n\t"
    "s_waitcnt vmcnt(10)\n\t"
    "s_add_u32 %[so], %[bw], 163840\n\t"
    "buffer_load_dwordx4 a[228:231], %[ga], %[sw], %[so] offen offset:0 nt\n\t"
    "buffer_load_dwordx4 a[232:235], %[ga], %[sw], %[so] offen offset:1024 nt\n\t"
    "buffer_load_dwordx4 a[48:51], %[ga], %[sw], %[so] offen offset:2048 nt\n\t"
    "buffer_load_dwordx4 a[52:55], %[ga], %[sw], %[so] offen offset:3072 nt\n\t"
    "s_add_u32 %[so], %[bw], 167936\n\t"
    "buffer_load_dwordx4 a[56:59], %[ga], %[sw], %[so] offen offset:0 nt\n\t"
    "buffer_load_dwordx4 a[60:63], %[ga], %[sw], %[so] offen offset:1024 nt\n\t"
    "buffer_load_dwordx4 a[64:67], %[ga], %[sw], %[so] offen offset:2048 nt\n\t"
    "buffer_load_dwordx4 a[68:71], %[ga], %[sw], %[so] offen offset:3072 nt\n\t"
    "s_add_u32 %[so], %[bw], 172032\n\t"
    "buffer_load_dwordx4 a[72:75], %[ga], %[sw], %[so] offen offset:0 nt\n\t"
    "buffer_load_dwordx4 a[76:79], %[ga], %[sw], %[so] offen offset:1024 nt\n\t"
    "ds_write_b128 %[wa10], a[196:199] offset:0\n\t"
    "ds_write_b128 %[wa10], a[200:203] offset:1024\n\t"
    "ds_write_b128 %[wa10], a[244:247] offset:2048\n\t"
    "ds_write_b128 %[wa10], a[168:171] offset:3072\n\t"
    "ds_write_b128 %[wa10], a[172:175] offset:4096\n\t"
    "ds_write_b128 %[wa10], a[176:179] offset:5120\n\t"
    "ds_write_b128 %[wa10], a[180:183] offset:6144\n\t"
    "ds_write_b128 %[wa10], a[184:187] offset:7168\n\t"
    "ds_write_b128 %[wa10], a[188:191] offset:8192\n\t"
    "ds_write_b128 %[wa10], a[192:195] offset:9216\n\t"
    "s_waitcnt lgkmcnt(0)\n\t"
    "s_barrier\n\t"
    "ds_read_b64_tr_b16 a[196:197], %[r32] offset:0\n\t"
    "ds_read_b64_tr_b16 a[198:199], %[r32] offset:5120\n\t"
    "ds_read_b64_tr_b16 a[200:201], %[r32] offset:32\n\t"
    "ds_read_b64_tr_b16 a[202:203], %[r32] offset:5152\n\t"
    "ds_read_b64_tr_b16 a[244:245], %[r32] offset:64\n\t"
    "ds_read_b64_tr_b16 a[246:247], %[r32] offset:5184\n\t"
    "ds_read_b64_tr_b16 a[168:169], %[r32] offset:96\n\t"
    "ds_read_b64_tr_b16 a[170:171], %[r32] offset:5216\n\t"
    "ds_read_b64_tr_b16 a[172:173], %[r32] offset:128\n\t"
    "ds_read_b64_tr_b16 a[174:175], %[r32] offset:5248\n\t"
    "ds_read_b64_tr_b16 a[176:177], %[r32] offset:160\n\t"
    "ds_read_b64_tr_b16 a[178:179], %[r32] offset:5280\n\t"
    "ds_read_b64_tr_b16 a[180:181], %[r32] offset:192\n\t"
    "ds_read_b64_tr_b16 a[182:183], %[r32] offset:5312\n\t"
    "ds_read_b64_tr_b16 a[184:185], %[r32] offset:224\n\t"
    "ds_read_b64_tr_b16 a[186:187], %[r32] offset:5344\n\t"
    "ds_read_b64_tr_b16 a[188:189], %[r32] offset:256\n\t"
    "ds_read_b64_tr_b16 a[190:191], %[r32] offset:5376\n\t"
    "ds_read_b64_tr_b16 a[192:193], %[r32] offset:288\n\t"
    "ds_read_b64_tr_b16 a[194:195], %[r32] offset:5408\n\t"
    "s_waitcnt vmcnt(10)\n\t"
    "s_add_u32 %[so], %[bw], 204800\n\t"
    "buffer_load_dwordx4 a[236:239], %[ga], %[sw], %[so] offen offset:0 nt\n\t"
    "buffer_load_dwordx4 a[240:243], %[ga], %[sw], %[so] offen offset:1024 nt\n\t"
    "buffer_load_dwordx4 a[16:19], %[ga], %[sw], %[so] offen offset:2048 nt\n\t"
    "buffer_load_dwordx4 a[20:23], %[ga], %[sw], %[so] offen offset:3072 nt\n\t"
    "s_add_u32 %[so], %[bw], 208896\n\t"
    "buffer_load_dwordx4 a[24:27], %[ga], %[sw], %[so] offen offset:0 nt\n\t"
    "buffer_load_dwordx4 a[28:31], %[ga], %[sw], %[so] offen offset:1024 nt\n\t"
    "buffer_load_dwordx4 a[32:35], %[ga], %[sw], %[so] offen offset:2048 nt\n\t"
    "buffer_load_dwordx4 a[36:39], %[ga], %[sw], %[so] offen offset:3072 nt\n\t"
    "s_add_u32 %[so], %[bw], 212992\n\t"
    "buffer_load_dwordx4 a[40:43], %[ga], %[sw], %[so] offen offset:0 nt\n\t"
    "buffer_load_dwordx4 a[44:47], %[ga], %[sw], %[so] offen offset:1024 nt\n\t"
    "ds_write_b128 %[wa10b], a[220:223] offset:0\n\t"
    "ds_write_b128 %[wa10b], a[224:227] offset:1024\n\t"
    "ds_write_b128 %[wa10b], a[80:83] offset:2048\n\t"
    "ds_write_b128 %[wa10b], a[84:87] offset:3072\n\t"
    "ds_write_b128 %[wa10b], a[88:91] offset:4096\n\t"
    "ds_write_b128 %[wa10b], a[92:95] offset:5120\n\t"
    "ds_write_b128 %[wa10b], a[96:99] offset:6144\n\t"
    "ds_write_b128 %[wa10b], a[100:103] offset:7168\n\t"
    "ds_write_b128 %[wa10b], a[104:107] offset:8192\n\t"
    "ds_write_b128 %[wa10b], a[108:111] offset:9216\n\t"
    "s_waitcnt lgkmcnt(0)\n\t"
    "s_barrier\n\t"
    "ds_read_b64_tr_b16 a[220:221], %[r32b] offset:0\n\t"
    "ds_read_b64_tr_b16 a[222:223], %[r32b] offset:5120\n\t"
    "ds_read_b64_tr_b16 a[224:225], %[r32b] offset:32\n\t"
    "ds_read_b64_tr_b16 a[226:227], %[r32b] offset:5152\n\t"
    "ds_read_b64_tr_b16 a[80:81], %[r32b] offset:64\n\t"
    "ds_read_b64_tr_b16 a[82:83], %[r32b] offset:5184\n\t"
    "ds_read_b64_tr_b16 a[84:85], %[r32b] offset:96\n\t"
    "ds_read_b64_tr_b16 a[86:87], %[r32b] offset:5216\n\t"
    "ds_read_b64_tr_b16 a[88:89], %[r32b] offset:128\n\t"
    "ds_read_b64_tr_b16 a[90:91], %[r32b] offset:5248\n\t"
    "ds_read_b64_tr_b16 a[92:93], %[r32b] offset:160\n\t"
    "ds_read_b64_tr_b16 a[94:95], %[r32b] offset:5280\n\t"
    "ds_read_b64_tr_b16 a[96:97], %[r32b] offset:192\n\t"
    "ds_read_b64_tr_b16 a[98:99], %[r32b] offset:5312\n\t"
    "ds_read_b64_tr_b16 a[100:101], %[r32b] offset:224\n\t"
    "ds_read_b64_tr_b16 a[102:103], %[r32b] offset:5344\n\t"
    "ds_read_b64_tr_b16 a[104:105], %[r32b] offset:256\n\t"
    "ds_read_b64_tr_b16 a[106:107], %[r32b] offset:5376\n\t"
    "ds_read_b64_tr_b16 a[108:109], %[r32b] offset:288\n\t"
    "ds_read_b64_tr_b16 a[110:111], %[r32b] offset:5408\n\t"
    "s_waitcnt vmcnt(10)\n\t"
    "s_add_u32 %[so], %[bg], 0\n\t"
    "buffer_load_dwordx4 v[16:19], %[g0], %[sg], %[so] offen nt\n\t"
    "s_add_u32 %[so], %[bg], 0\n\t"
    "buffer_load_dwordx4 v[20:23], %[g1], %[sg], %[so] offen nt\n\t"
    "s_add_u32 %[so], %[bg], 0\n\t"
    "buffer_load_dwordx4 v[24:27], %[g2], %[sg], %[so] offen nt\n\t"
    "s_add_u32 %[so], %[bg], 0\n\t"
    "buffer_load_dwordx4 v[28:31], %[g0], %[su], %[so] offen nt\n\t"
    "s_add_u32 %[so], %[bg], 0\n\t"
    "buffer_load_dwordx4 v[32:35], %[g1], %[su], %[so] offen nt\n\t"
    "s_add_u32 %[so], %[bg], 0\n\t"
    "buffer_load_dwordx4 v[36:39], %[g2], %[su], %[so] offen nt\n\t"
    "s_add_u32 %[so], %[bg], 98304\n\t"
    "buffer_load_dwordx4 v[40:43], %[g0], %[sg], %[so] offen nt\n\t"
    "s_add_u32 %[so], %[bg], 98304\n\t"
    "buffer_load_dwordx4 v[44:47], %[g1], %[sg], %[so] offen nt\n\t"
    "s_add_u32 %[so], %[bg], 98304\n\t"
    "buffer_load_dwordx4 v[48:51], %[g2], %[sg], %[so] offen nt\n\t"
    "s_add_u32 %[so], %[bg], 98304\n\t"
    "buffer_load_dwordx4 v[52:55], %[g0], %[su], %[so] offen nt\n\t"
    "ds_write_b128 %[wa10], a[228:231] offset:0\n\t"
    "ds_write_b128 %[wa10], a[232:235] offset:1024\n\t"
    "ds_write_b128 %[wa10], a[48:51] offset:2048\n\t"
    "ds_write_b128 %[wa10], a[52:55] offset:3072\n\t"
    "ds_write_b128 %[wa10], a[56:59] offset:4096\n\t"
    "ds_write_b128 %[wa10], a[60:63] offset:5120\n\t"
    "ds_write_b128 %[wa10], a[64:67] offset:6144\n\t"
    "ds_write_b128 %[wa10], a[68:71] offset:7168\n\t"
    "ds_write_b128 %[wa10], a[72:75] offset:8192\n\t"
    "ds_write_b128 %[wa10], a[76:79] offset:9216\n\t"
    "s_waitcnt lgkmcnt(0)\n\t"
    "s_barrier\n\t"
    "ds_read_b64_tr_b16 a[228:229], %[r32] offset:0\n\t"
    "ds_read_b64_tr_b16 a[230:231], %[r32] offset:5120\n\t"
    "ds_read_b64_tr_b16 a[232:233], %[r32] offset:32\n\t"
    "ds_read_b64_tr_b16 a[234:235], %[r32] offset:5152\n\t"
    "ds_read_b64_tr_b16 a[48:49], %[r32] offset:64\n\t"
    "ds_read_b64_tr_b16 a[50:51], %[r32] offset:5184\n\t"
    "ds_read_b64_tr_b16 a[52:53], %[r32] offset:96\n\t"
    "ds_read_b64_tr_b16 a[54:55], %[r32] offset:5216\n\t"
    "ds_read_b64_tr_b16 a[56:57], %[r32] offset:128\n\t"
    "ds_read_b64_tr_b16 a[58:59], %[r32] offset:5248\n\t"
    "ds_read_b64_tr_b16 a[60:61], %[r32] offset:160\n\t"
    "ds_read_b64_tr_b16 a[62:63], %[r32] offset:5280\n\t"
    "ds_read_b64_tr_b16 a[64:65], %[r32] offset:192\n\t"
    "ds_read_b64_tr_b16 a[66:67], %[r32] offset:5312\n\t"
    "ds_read_b64_tr_b16 a[68:69], %[r32] offset:224\n\t"
    "ds_read_b64_tr_b16 a[70:71], %[r32] offset:5344\n\t"
    "ds_read_b64_tr_b16 a[72:73], %[r32] offset:256\n\t"
    "ds_read_b64_tr_b16 a[74:75], %[r32] offset:5376\n\t"
    "ds_read_b64_tr_b16 a[76:77], %[r32] offset:288\n\t"
    "ds_read_b64_tr_b16 a[78:79], %[r32] offset:5408\n\t"
    "s_waitcnt vmcnt(10)\n\t"
    "ds_write_b128 %[wa10b], a[236:239] offset:0\n\t"
    "ds_write_b128 %[wa10b], a[240:243] offset:1024\n\t"
    "ds_write_b128 %[wa10b], a[16:19] offset:2048\n\t"
    "ds_write_b128 %[wa10b], a[20:23] offset:3072\n\t"
    "ds_write_b128 %[wa10b], a[24:27] offset:4096\n\t"
    "ds_write_b128 %[wa10b], a[28:31] offset:5120\n\t"
    "ds_write_b128 %[wa10b], a[32:35] offset:6144\n\t"
    "ds_write_b128 %[wa10b], a[36:39] offset:7168\n\t"
    "ds_write_b128 %[wa10b], a[40:43] offset:8192\n\t"
    "ds_write_b128 %[wa10b], a[44:47] offset:9216\n\t"
    "s_waitcnt lgkmcnt(0)\n\t"
    "s_barrier\n\t"
    "ds_read_b64_tr_b16 a[236:237], %[r32b] offset:0\n\t"
    "ds_read_b64_tr_b16 a[238:239], %[r32b] offset:5120\n\t"
    "ds_read_b64_tr_b16 a[240:241], %[r32b] offset:32\n\t"
    "ds_read_b64_tr_b16 a[242:243], %[r32b] offset:5152\n\t"
    "ds_read_b64_tr_b16 a[16:17], %[r32b] offset:64\n\t"
    "ds_read_b64_tr_b16 a[18:19], %[r32b] offset:5184\n\t"
    "ds_read_b64_tr_b16 a[20:21], %[r32b] offset:96\n\t"
    "ds_read_b64_tr_b16 a[22:23], %[r32b] offset:5216\n\t"
    "ds_read_b64_tr_b16 a[24:25], %[r32b] offset:128\n\t"
    "ds_read_b64_tr_b16 a[26:27], %[r32b] offset:5248\n\t"
    "ds_read_b64_tr_b16 a[28:29], %[r32b] offset:160\n\t"
    "ds_read_b64_tr_b16 a[30:31], %[r32b] offset:5280\n\t"
    "ds_read_b64_tr_b16 a[32:33], %[r32b] offset:192\n\t"
    "ds_read_b64_tr_b16 a[34:35], %[r32b] offset:5312\n\t"
    "ds_read_b64_tr_b16 a[36:37], %[r32b] offset:224\n\t"
    "ds_read_b64_tr_b16 a[38:39], %[r32b] offset:5344\n\t"
    "ds_read_b64_tr_b16 a[40:41], %[r32b] offset:256\n\t"
    "ds_read_b64_tr_b16 a[42:43], %[r32b] offset:5376\n\t"
    "ds_read_b64_tr_b16 a[44:45], %[r32b] offset:288\n\t"
    "ds_read_b64_tr_b16 a[46:47], %[r32b] offset:5408\n\t"
    "s_waitcnt lgkmcnt(0)\n\t"
    "s_barrier\n\t"
    "s_waitcnt vmcnt(9)\n\t"
    "ds_write_b128 %[l0], v[16:19] offset:0\n\t"
    "s_waitcnt lgkmcnt(0)\n\t"
    "s_add_u32 %[so], %[bg], 98304\n\t"
    "buffer_load_dwordx4 v[16:19], %[g1], %[su], %[so] offen nt\n\t"
    "s_waitcnt vmcnt(9)\n\t"
    "ds_write_b128 %[l1], v[20:23] offset:0\n\t"
    "s_waitcnt lgkmcnt(0)\n\t"
    "s_add_u32 %[so], %[bg], 98304\n\t"
    "buffer_load_dwordx4 v[20:23], %[g2], %[su], %[so] offen nt\n\t"
    "s_waitcnt vmcnt(9)\n\t"
    "ds_write_b128 %[l2], v[24:27] offset:0\n\t"
    "s_waitcnt lgkmcnt(0)\n\t"
    "s_add_u32 %[so], %[bg], 196608\n\t"
    "buffer_load_dwordx4 v[24:27], %[g0], %[sg], %[so] offen nt\n\t"
    "s_waitcnt vmcnt(9)\n\t"
    "ds_write_b128 %[l0], v[28:31] offset:3072\n\t"
    "s_waitcnt lgkmcnt(0)\n\t"
    "s_add_u32 %[so], %[bg], 196608\n\t"
    "buffer_load_dwordx4 v[28:31], %[g1], %[sg], %[so] offen nt\n\t"
    "s_waitcnt vmcnt(9)\n\t"
    "ds_write_b128 %[l1], v[32:35] offset:3072\n\t"
    "s_waitcnt lgkmcnt(0)\n\t"
    "s_add_u32 %[so], %[bg], 196608\n\t"
    "buffer_load_dwordx4 v[32:35], %[g2], %[sg], %[so] offen nt\n\t"
    "s_waitcnt vmcnt(9)\n\t"
    "ds_write_b128 %[l2], v[36:39] offset:3072\n\t"
    "s_waitcnt lgkmcnt(0)\n\t"
    "s_add_u32 %[so], %[bg], 196608\n\t"
    "buffer_load_dwordx4 v[36:39], %[g0], %[su], %[so] offen nt\n\t"
    "s_waitcnt vmcnt(9)\n\t"
    "ds_write_b128 %[l0], v[40:43] offset:6144\n\t"
    "s_waitcnt lgkmcnt(0)\n\t"
    "s_add_u32 %[so], %[bg], 196608\n\t"
    "buffer_load_dwordx4 v[40:43], %[g1], %[su], %[so] offen nt\n\t"
    "s_waitcnt vmcnt(9)\n\t"
    "ds_write_b128 %[l1], v[44:47] offset:6144\n\t"
    "s_waitcnt lgkmcnt(0)\n\t"
    "s_add_u32 %[so], %[bg], 196608\n\t"
    "buffer_load_dwordx4 v[44:47], %[g2], %[su], %[so] offen nt\n\t"
    "s_waitcnt vmcnt(9)\n\t"
    "ds_write_b128 %[l2], v[48:51] offset:6144\n\t"
    "s_waitcnt lgkmcnt(0)\n\t"
    "s_add_u32 %[so], %[bg], 294912\n\t"
    "buffer_load_dwordx4 v[48:51], %[g0], %[sg], %[so] offen nt\n\t"
    "s_waitcnt vmcnt(9)\n\t"
    "ds_write_b128 %[l0], v[52:55] offset:9216\n\t"
    "s_waitcnt lgkmcnt(0)\n\t"
    "s_add_u32 %[so], %[bg], 294912\n\t"
    "buffer_load_dwordx4 v[52:55], %[g1], %[sg], %[so] offen nt\n\t"
    "s_waitcnt vmcnt(9)\n\t"
    "ds_write_b128 %[l1], v[16:19] offset:9216\n\t"
    "s_waitcnt lgkmcnt(0)\n\t"
    "s_add_u32 %[so], %[bg], 294912\n\t"
    "buffer_load_dwordx4 v[16:19], %[g2], %[sg], %[so] offen nt\n\t"
    "s_waitcnt vmcnt(9)\n\t"
    "ds_write_b128 %[l2], v[20:23] offset:9216\n\t"
    "s_waitcnt lgkmcnt(0)\n\t"
    "s_add_u32 %[so], %[bg], 294912\n\t"
    "buffer_load_dwordx4 v[20:23], %[g0], %[su], %[so] offen nt\n\t"
    "s_waitcnt vmcnt(9)\n\t"
    "ds_write_b128 %[l0], v[24:27] offset:12288\n\t"
    "s_waitcnt lgkmcnt(0)\n\t"
    "s_add_u32 %[so], %[bg], 294912\n\t"
    "buffer_load_dwordx4 v[24:27], %[g1], %[su], %[so] offen nt\n\t"
    "s_waitcnt vmcnt(9)\n\t"
    "ds_write_b128 %[l1], v[28:31] offset:12288\n\t"
    "s_waitcnt lgkmcnt(0)\n\t"
    "s_add_u32 %[so], %[bg], 294912\n\t"
    "buffer_load_dwordx4 v[28:31], %[g2], %[su], %[so] offen nt\n\t"
    "s_waitcnt vmcnt(9)\n\t"
    "ds_write_b128 %[l2], v[32:35] offset:12288\n\t"
    "s_waitcnt lgkmcnt(0)\n\t"
    "s_add_u32 %[so], %[bg], 393216\n\t"
    "buffer_load_dwordx4 v[32:35], %[g0], %[sg], %[so] offen nt\n\t"
    "s_waitcnt vmcnt(9)\n\t"
    "ds_write_b128 %[l0], v[36:39] offset:15360\n\t"
    "s_waitcnt lgkmcnt(0)\n\t"
    "s_add_u32 %[so], %[bg], 393216\n\t"
    "buffer_load_dwordx4 v[36:39], %[g1], %[sg], %[so] offen nt\n\t"
    "s_waitcnt vmcnt(9)\n\t"
    "ds_write_b128 %[l1], v[40:43] offset:15360\n\t"
    "s_waitcnt lgkmcnt(0)\n\t"
    "s_add_u32 %[so], %[bg], 393216\n\t"
    "buffer_load_dwordx4 v[40:43], %[g2], %[sg], %[so] offen nt\n\t"
    "s_waitcnt vmcnt(9)\n\t"
    "ds_write_b128 %[l2], v[44:47] offset:15360\n\t"
    "s_waitcnt lgkmcnt(0)\n\t"
    "s_add_u32 %[so], %[bg], 393216\n\t"
    "buffer_load_dwordx4 v[44:47], %[g0], %[su], %[so] offen nt\n\t"
    "s_waitcnt vmcnt(9)\n\t"
    "ds_write_b128 %[l0], v[48:51] offset:18432\n\t"
    "s_waitcnt lgkmcnt(0)\n\t"
    "s_add_u32 %[so], %[bg], 393216\n\t"
    "buffer_load_dwordx4 v[48:51], %[g1], %[su], %[so] offen nt\n\t"
    "s_waitcnt vmcnt(9)\n\t"
    "ds_write_b128 %[l1], v[52:55] offset:18432\n\t"
    "s_waitcnt lgkmcnt(0)\n\t"
    "s_add_u32 %[so], %[bg], 393216\n\t"
    "buffer_load_dwordx4 v[52:55], %[g2], %[su], %[so] offen nt\n\t"
    "s_waitcnt vmcnt(9)\n\t"
    "ds_write_b128 %[l2], v[16:19] offset:18432\n\t"
    "s_waitcnt vmcnt(8)\n\t"
    "ds_write_b128 %[l0], v[20:23] offset:21504\n\t"
    "s_waitcnt vmcnt(7)\n\t"
    "ds_write_b128 %[l1], v[24:27] offset:21504\n\t"
    "s_waitcnt vmcnt(6)\n\t"
    "ds_write_b128 %[l2], v[28:31] offset:21504\n\t"
    "s_waitcnt vmcnt(5)\n\t"
    "ds_write_b128 %[l0], v[32:35] offset:24576\n\t"
    "s_waitcnt vmcnt(4)\n\t"
    "ds_write_b128 %[l1], v[36:39] offset:24576\n\t"
    "s_waitcnt vmcnt(3)\n\t"
    "ds_write_b128 %[l2], v[40:43] offset:24576\n\t"
    "s_waitcnt vmcnt(2)\n\t"
    "ds_write_b128 %[l0], v[44:47] offset:27648\n\t"
    "s_waitcnt vmcnt(1)\n\t"
    "ds_write_b128 %[l1], v[48:51] offset:27648\n\t"
    "s_waitcnt vmcnt(0)\n\t"
    "ds_write_b128 %[l2], v[52:55] offset:27648\n\t"
    "s_waitcnt vmcnt(0) lgkmcnt(0)\n\t"
      : [so] "=&s"(so)
      : [ga] "v"(ga), [wa10] "v"(wa10), [wa10b] "v"(wa10b), [r32] "v"(r32), [r32b] "v"(r32b), [sw] "s"(srd_wo), [bw] "s"(bw),
        [g0] "v"(g[0]), [g1] "v"(g[1]), [g2] "v"(g[2]), [l0] "v"(l[0]), [l1] "v"(l[1]), [l2] "v"(l[2]), [sg] "s"(sg), [su] "s"(su), [bg] "s"(bg) : "memory", "scc", TCLOB);
  }
  {
    LANE();
#pragma unroll
    for (int i = 0; i < 5; ++i) { lds_w32(YS_OFF + 256 * i + 4 * lane, 0x3f803f80u); lds_w32(YS_OFF + 1280 + 256 * i + 4 * lane, 0u); }
    float o0, o1, o2;
    { const unsigned ya = YS_OFF + 1280 * (j & 1) + 16 * c, ia = WQI_OFF + 6144 * w + 16 * lane;
      asm volatile(
    "ds_read_b128 v[16:19], %[ia] offset:0\n\t"
    "ds_read_b128 v[20:23], %[ia] offset:1024\n\t"
    "ds_read_b128 v[24:27], %[ia] offset:2048\n\t"
    "ds_read_b128 v[28:31], %[ya] offset:1152\n\t"
    "ds_read_b128 v[32:35], %[ya] offset:0\n\t"
    "ds_read_b128 v[36:39], %[ya] offset:64\n\t"
    "s_waitcnt lgkmcnt(2)\n\t"
    "v_mfma_f32_16x16x32_bf16 v[40:43], v[28:31], v[16:19], 0\n\t"
    "v_mfma_f32_16x16x32_bf16 v[44:47], v[28:31], v[20:23], 0\n\t"
    "v_mfma_f32_16x16x32_bf16 v[48:51], v[28:31], v[24:27], 0\n\t"
    "ds_read_b128 v[16:19], %[ia] offset:3072\n\t"
    "ds_read_b128 v[20:23], %[ia] offset:4096\n\t"
    "ds_read_b128 v[24:27], %[ia] offset:5120\n\t"
    "ds_read_b128 v[28:31], %[ya] offset:1216\n\t"
    "s_waitcnt lgkmcnt(5)\n\t"
    "v_mfma_f32_16x16x32_bf16 v[40:43], v[32:35], v[56:59], v[40:43]\n\t"
    "v_mfma_f32_16x16x32_bf16 v[44:47], v[32:35], v[60:63], v[44:47]\n\t"
    "v_mfma_f32_16x16x32_bf16 v[48:51], v[32:35], v[64:67], v[48:51]\n\t"
    "s_waitcnt lgkmcnt(4)\n\t"
    "v_mfma_f32_16x16x32_bf16 v[40:43], v[36:39], v[68:71], v[40:43]\n\t"
    "v_mfma_f32_16x16x32_bf16 v[44:47], v[36:39], v[72:75], v[44:47]\n\t"
    "v_mfma_f32_16x16x32_bf16 v[48:51], v[36:39], v[76:79], v[48:51]\n\t"
    "ds_read_b128 v[32:35], %[ya] offset:128\n\t"
    "ds_read_b128 v[36:39], %[ya] offset:192\n\t"
    "s_waitcnt lgkmcnt(2)\n\t"
    "v_mfma_f32_16x16x32_bf16 v[40:43], v[28:31], v[16:19], v[40:43]\n\t"
    "v_mfma_f32_16x16x32_bf16 v[44:47], v[28:31], v[20:23], v[44:47]\n\t"
    "v_mfma_f32_16x16x32_bf16 v[48:51], v[28:31], v[24:27], v[48:51]\n\t"
    "ds_read_b128 v[16:19], %[ya] offset:256\n\t"
    "ds_read_b128 v[20:23], %[ya] offset:320\n\t"
    "ds_read_b128 v[24:27], %[ya] offset:384\n\t"
    "ds_read_b128 v[28:31], %[ya] offset:448\n\t"
    "s_waitcnt lgkmcnt(5)\n\t"
    "v_mfma_f32_16x16x32_bf16 v[40:43], v[32:35], v[80:83], v[40:43]\n\t"
    "v_mfma_f32_16x16x32_bf16 v[44:47], v[32:35], v[84:87], v[44:47]\n\t"
    "v_mfma_f32_16x16x32_bf16 v[48:51], v[32:35], v[88:91], v[48:51]\n\t"
    "ds_read_b128 v[32:35], %[ya] offset:512\n\t"
    "s_waitcnt lgkmcnt(5)\n\t"
    "v_mfma_f32_16x16x32_bf16 v[40:43], v[36:39], v[92:95], v[40:43]\n\t"
    "v_mfma_f32_16x16x32_bf16 v[44:47], v[36:39], v[96:99], v[44:47]\n\t"
    "v_mfma_f32_16x16x32_bf16 v[48:51], v[36:39], v[100:103], v[48:51]\n\t"
    "ds_read_b128 v[36:39], %[ya] offset:576\n\t"
    "s_waitcnt lgkmcnt(5)\n\t"
    "v_mfma_f32_16x16x32_bf16 v[40:43], v[16:19], v[104:107], v[40:43]\n\t"
    "v_mfma_f32_16x16x32_bf16 v[44:47], v[16:19], v[108:111], v[44:47]\n\t"
    "v_mfma_f32_16x16x32_bf16 v[48:51], v[16:19], v[112:115], v[48:51]\n\t"
    "ds_read_b128 v[16:19], %[ya] offset:640\n\t"
    "s_waitcnt lgkmcnt(5)\n\t"
    "v_mfma_f32_16x16x32_bf16 v[40:43], v[20:23], v[116:119], v[40:43]\n\t"
    "v_mfma_f32_16x16x32_bf16 v[44:47], v[20:23], v[120:123], v[44:47]\n\t"
    "v_mfma_f32_16x16x32_bf16 v[48:51], v[20:23], v[124:127], v[48:51]\n\t"
    "ds_read_b128 v[20:23], %[ya] offset:704\n\t"
    "s_waitcnt lgkmcnt(5)\n\t"
    "v_mfma_f32_16x16x32_bf16 v[40:43], v[24:27], v[128:131], v[40:43]\n\t"
    "v_mfma_f32_16x16x32_bf16 v[44:47], v[24:27], v[132:135], v[44:47]\n\t"
    "v_mfma_f32_16x16x32_bf16 v[48:51], v[24:27], v[136:139], v[48:51]\n\t"
    "ds_read_b128 v[24:27], %[ya] offset:768\n\t"
    "s_waitcnt lgkmcnt(5)\n\t"
    "v_mfma_f32_16x16x32_bf16 v[40:43], v[28:31], v[140:143], v[40:43]\n\t"
    "v_mfma_f32_16x16x32_bf16 v[44:47], v[28:31], v[144:147], v[44:47]\n\t"
    "v_mfma_f32_16x16x32_bf16 v[48:51], v[28:31], v[148:151], v[48:51]\n\t"
    "ds_read_b128 v[28:31], %[ya] offset:832\n\t"
    "s_waitcnt lgkmcnt(5)\n\t"
    "v_mfma_f32_16x16x32_bf16 v[40:43], v[32:35], v[152:155], v[40:43]\n\t"
    "v_mfma_f32_16x16x32_bf16 v[44:47], v[32:35], v[156:159], v[44:47]\n\t"
    "v_mfma_f32_16x16x32_bf16 v[48:51], v[32:35], v[160:163], v[48:51]\n\t"
    "ds_read_b128 v[32:35], %[ya] offset:896\n\t"
    "s_waitcnt lgkmcnt(5)\n\t"
    "v_mfma_f32_16x16x32_bf16 v[40:43], v[36:39], v[164:167], v[40:43]\n\t"
    "v_mfma_f32_16x16x32_bf16 v[44:47], v[36:39], v[168:171], v[44:47]\n\t"
    "v_mfma_f32_16x16x32_bf16 v[48:51], v[36:39], v[172:175], v[48:51]\n\t"
    "ds_read_b128 v[36:39], %[ya] offset:960\n\t"
    "s_waitcnt lgkmcnt(5)\n\t"
    "v_mfma_f32_16x16x32_bf16 v[40:43], v[16:19], v[176:179], v[40:43]\n\t"
    "v_mfma_f32_16x16x32_bf16 v[44:47], v[16:19], v[180:183], v[44:47]\n\t"
    "v_mfma_f32_16x16x32_bf16 v[48:51], v[16:19], v[184:187], v[48:51]\n\t"
    "ds_read_b128 v[16:19], %[ya] offset:1024\n\t"
    "s_waitcnt lgkmcnt(5)\n\t"
    "v_mfma_f32_16x16x32_bf16 v[40:43], v[20:23], v[188:191], v[40:43]\n\t"
    "v_mfma_f32_16x16x32_bf16 v[44:47], v[20:23], v[192:195], v[44:47]\n\t"
    "v_mfma_f32_16x16x32_bf16 v[48:51], v[20:23], v[196:199], v[48:51]\n\t"
    "ds_read_b128 v[20:23], %[ya] offset:1088\n\t"
    "s_waitcnt lgkmcnt(5)\n\t"
    "v_mfma_f32_16x16x32_bf16 v[40:43], v[24:27], v[200:203], v[40:43]\n\t"
    "v_mfma_f32_16x16x32_bf16 v[44:47], v[24:27], v[204:207], v[44:47]\n\t"
    "v_mfma_f32_16x16x32_bf16 v[48:51], v[24:27], v[208:211], v[48:51]\n\t"
    "s_waitcnt lgkmcnt(4)\n\t"
    "v_mfma_f32_16x16x32_bf16 v[40:43], v[28:31], v[212:215], v[40:43]\n\t"
    "v_mfma_f32_16x16x32_bf16 v[44:47], v[28:31], v[216:219], v[44:47]\n\t"
    "v_mfma_f32_16x16x32_bf16 v[48:51], v[28:31], v[220:223], v[48:51]\n\t"
    "s_waitcnt lgkmcnt(3)\n\t"
    "v_mfma_f32_16x16x32_bf16 v[40:43], v[32:35], v[224:227], v[40:43]\n\t"
    "v_mfma_f32_16x16x32_bf16 v[44:47], v[32:35], v[228:231], v[44:47]\n\t"
    "v_mfma_f32_16x16x32_bf16 v[48:51], v[32:35], v[232:235], v[48:51]\n\t"
    "s_waitcnt lgkmcnt(2)\n\t"
    "v_mfma_f32_16x16x32_bf16 v[40:43], v[36:39], v[236:239], v[40:43]\n\t"
    "v_mfma_f32_16x16x32_bf16 v[44:47], v[36:39], v[240:243], v[44:47]\n\t"
    "v_mfma_f32_16x16x32_bf16 v[48:51], v[36:39], v[244:247], v[48:51]\n\t"
    "s_waitcnt lgkmcnt(1)\n\t"
    "v_mfma_f32_16x16x32_bf16 v[40:43], v[16:19], v[248:251], v[40:43]\n\t"
    "v_mfma_f32_16x16x32_bf16 v[44:47], v[16:19], v[252:255], v[44:47]\n\t"
    "v_mfma_f32_16x16x32_bf16 v[48:51], v[16:19], a[0:3], v[48:51]\n\t"
    "s_waitcnt lgkmcnt(0)\n\t"
    "v_mfma_f32_16x16x32_bf16 v[40:43], v[20:23], a[4:7], v[40:43]\n\t"
    "v_mfma_f32_16x16x32_bf16 v[44:47], v[20:23], a[8:11], v[44:47]\n\t"
    "v_mfma_f32_16x16x32_bf16 v[48:51], v[20:23], a[12:15], v[48:51]\n\t"
    "s_nop 7\n\t"
    "v_add_f32 %[o0], v40, v41\n\t"
    "v_add_f32 %[o1], v44, v45\n\t"
    "v_add_f32 %[o2], v48, v49\n\t"
        : [o0] "=&v"(o0), [o1] "=&v"(o1), [o2] "=&v"(o2) : [ya] "v"(ya), [ia] "v"(ia) : "memory", TCLOB); }
    lds_w32(SQ_OFF + 192 * w + 4 * j, __float_as_uint(o0)); lds_w32(SQ_OFF + 192 * w + 64 + 4 * j, __float_as_uint(o1)); lds_w32(SQ_OFF + 192 * w + 128 + 4 * j, __float_as_uint(o2));
  }
  {
    LANE();
    float gp, up;
    const unsigned sw = (c & 1) << 2;
    const unsigned r0 = stage + ((8 * c + (j >> 2)) ^ sw) * 32 + 8 * (j & 3), r1 = stage + ((8 * c + 4 + (j >> 2)) ^ sw) * 32 + 8 * (j & 3);
    const unsigned ya = YS_OFF + 1280 * (j & 1) + 16 * c + 320 * w;
    asm volatile(
    "ds_read_b128 v[28:31], %[ya] offset:0\n\t"
    "ds_read_b64_tr_b16 v[36:37], %[r0] offset:0\n\t"
    "ds_read_b64_tr_b16 v[38:39], %[r1] offset:0\n\t"
    "ds_read_b64_tr_b16 v[40:41], %[r0] offset:1024\n\t"
    "ds_read_b64_tr_b16 v[42:43], %[r1] offset:1024\n\t"
    "ds_read_b64_tr_b16 v[44:45], %[r0] offset:2048\n\t"
    "ds_read_b64_tr_b16 v[46:47], %[r1] offset:2048\n\t"
    "ds_read_b128 v[32:35], %[ya] offset:64\n\t"
    "ds_read_b64_tr_b16 v[48:49], %[r0] offset:6144\n\t"
    "ds_read_b64_tr_b16 v[50:51], %[r1] offset:6144\n\t"
    "ds_read_b64_tr_b16 v[52:53], %[r0] offset:7168\n\t"
    "ds_read_b64_tr_b16 v[54:55], %[r1] offset:7168\n\t"
    "s_waitcnt lgkmcnt(9)\n\t"
    "v_mfma_f32_16x16x32_bf16 v[16:19], v[28:31], v[36:39], 0\n\t"
    "ds_read_b64_tr_b16 v[36:37], %[r0] offset:8192\n\t"
    "ds_read_b64_tr_b16 v[38:39], %[r1] offset:8192\n\t"
    "s_waitcnt lgkmcnt(9)\n\t"
    "v_mfma_f32_16x16x32_bf16 v[20:23], v[28:31], v[40:43], 0\n\t"
    "ds_read_b64_tr_b16 v[40:41], %[r0] offset:12288\n\t"
    "ds_read_b64_tr_b16 v[42:43], %[r1] offset:12288\n\t"
    "s_waitcnt lgkmcnt(9)\n\t"
    "v_mfma_f32_16x16x32_bf16 v[24:27], v[28:31], v[44:47], 0\n\t"
    "ds_read_b64_tr_b16 v[44:45], %[r0] offset:13312\n\t"
    "ds_read_b64_tr_b16 v[46:47], %[r1] offset:13312\n\t"
    "ds_read_b128 v[28:31], %[ya] offset:128\n\t"
    "s_waitcnt lgkmcnt(9)\n\t"
    "v_mfma_f32_16x16x32_bf16 v[16:19], v[32:35], v[48:51], v[16:19]\n\t"
    "ds_read_b64_tr_b16 v[48:49], %[r0] offset:14336\n\t"
    "ds_read_b64_tr_b16 v[50:51], %[r1] offset:14336\n\t"
    "s_waitcnt lgkmcnt(9)\n\t"
    "v_mfma_f32_16x16x32_bf16 v[20:23], v[32:35], v[52:55], v[20:23]\n\t"
    "ds_read_b64_tr_b16 v[52:53], %[r0] offset:18432\n\t"
    "ds_read_b64_tr_b16 v[54:55], %[r1] offset:18432\n\t"
    "s_waitcnt lgkmcnt(9)\n\t"
    "v_mfma_f32_16x16x32_bf16 v[24:27], v[32:35], v[36:39], v[24:27]\n\t"
    "ds_read_b64_tr_b16 v[36:37], %[r0] offset:19456\n\t"
    "ds_read_b64_tr_b16 v[38:39], %[r1] offset:19456\n\t"
    "ds_read_b128 v[32:35], %[ya] offset:192\n\t"
    "s_waitcnt lgkmcnt(7)\n\t"
    "v_mfma_f32_16x16x32_bf16 v[16:19], v[28:31], v[40:43], v[16:19]\n\t"
    "ds_read_b64_tr_b16 v[40:41], %[r0] offset:20480\n\t"
    "ds_read_b64_tr_b16 v[42:43], %[r1] offset:20480\n\t"
    "v_mfma_f32_16x16x32_bf16 v[20:23], v[28:31], v[44:47], v[20:23]\n\t"
    "ds_read_b64_tr_b16 v[44:45], %[r0] offset:24576\n\t"
    "ds_read_b64_tr_b16 v[46:47], %[r1] offset:24576\n\t"
    "s_waitcnt lgkmcnt(9)\n\t"
    "v_mfma_f32_16x16x32_bf16 v[24:27], v[28:31], v[48:51], v[24:27]\n\t"
    "ds_read_b64_tr_b16 v[48:49], %[r0] offset:25600\n\t"
    "ds_read_b64_tr_b16 v[50:51], %[r1] offset:25600\n\t"
    "ds_read_b128 v[28:31], %[ya] offset:256\n\t"
    "s_waitcnt lgkmcnt(7)\n\t"
    "v_mfma_f32_16x16x32_bf16 v[16:19], v[32:35], v[52:55], v[16:19]\n\t"
    "ds_read_b64_tr_b16 v[52:53], %[r0] offset:26624\n\t"
    "ds_read_b64_tr_b16 v[54:55], %[r1] offset:26624\n\t"
    "v_mfma_f32_16x16x32_bf16 v[20:23], v[32:35], v[36:39], v[20:23]\n\t"
    "ds_read_b64_tr_b16 v[36:37], %[r0] offset:3072\n\t"
    "ds_read_b64_tr_b16 v[38:39], %[r1] offset:3072\n\t"
    "s_waitcnt lgkmcnt(9)\n\t"
    "v_mfma_f32_16x16x32_bf16 v[24:27], v[32:35], v[40:43], v[24:27]\n\t"
    "ds_read_b64_tr_b16 v[40:41], %[r0] offset:4096\n\t"
    "ds_read_b64_tr_b16 v[42:43], %[r1] offset:4096\n\t"
    "ds_read_b128 v[32:35], %[ya] offset:0\n\t"
    "s_waitcnt lgkmcnt(7)\n\t"
    "v_mfma_f32_16x16x32_bf16 v[16:19], v[28:31], v[44:47], v[16:19]\n\t"
    "ds_read_b64_tr_b16 v[44:45], %[r0] offset:5120\n\t"
    "ds_read_b64_tr_b16 v[46:47], %[r1] offset:5120\n\t"
    "v_mfma_f32_16x16x32_bf16 v[20:23], v[28:31], v[48:51], v[20:23]\n\t"
    "ds_read_b64_tr_b16 v[48:49], %[r0] offset:9216\n\t"
    "ds_read_b64_tr_b16 v[50:51], %[r1] offset:9216\n\t"
    "s_waitcnt lgkmcnt(9)\n\t"
    "v_mfma_f32_16x16x32_bf16 v[24:27], v[28:31], v[52:55], v[24:27]\n\t"
    "ds_read_b64_tr_b16 v[52:53], %[r0] offset:10240\n\t"
    "ds_read_b64_tr_b16 v[54:55], %[r1] offset:10240\n\t"
    "ds_read_b128 v[28:31], %[ya] offset:64\n\t"
    "s_nop 7\n\t"
    "v_add_f32 %[o0], v16, v17\n\t"
    "v_add_f32 v18, v20, v21\n\t"
    "v_cndmask_b32 %[o0], %[o0], v18, %[g1]\n\t"
    "v_add_f32 v18, v24, v25\n\t"
    "v_cndmask_b32 %[o0], %[o0], v18, %[g2]\n\t"
    "s_waitcnt lgkmcnt(7)\n\t"
    "v_mfma_f32_16x16x32_bf16 v[16:19], v[32:35], v[36:39], 0\n\t"
    "ds_read_b64_tr_b16 v[36:37], %[r0] offset:11264\n\t"
    "ds_read_b64_tr_b16 v[38:39], %[r1] offset:11264\n\t"
    "v_mfma_f32_16x16x32_bf16 v[20:23], v[32:35], v[40:43], 0\n\t"
    "ds_read_b64_tr_b16 v[40:41], %[r0] offset:15360\n\t"
    "ds_read_b64_tr_b16 v[42:43], %[r1] offset:15360\n\t"
    "s_waitcnt lgkmcnt(9)\n\t"
    "v_mfma_f32_16x16x32_bf16 v[24:27], v[32:35], v[44:47], 0\n\t"
    "ds_read_b64_tr_b16 v[44:45], %[r0] offset:16384\n\t"
    "ds_read_b64_tr_b16 v[46:47], %[r1] offset:16384\n\t"
    "ds_read_b128 v[32:35], %[ya] offset:128\n\t"
    "s_waitcnt lgkmcnt(7)\n\t"
    "v_mfma_f32_16x16x32_bf16 v[16:19], v[28:31], v[48:51], v[16:19]\n\t"
    "ds_read_b64_tr_b16 v[48:49], %[r0] offset:17408\n\t"
    "ds_read_b64_tr_b16 v[50:51], %[r1] offset:17408\n\t"
    "v_mfma_f32_16x16x32_bf16 v[20:23], v[28:31], v[52:55], v[20:23]\n\t"
    "ds_read_b64_tr_b16 v[52:53], %[r0] offset:21504\n\t"
    "ds_read_b64_tr_b16 v[54:55], %[r1] offset:21504\n\t"
    "s_waitcnt lgkmcnt(9)\n\t"
    "v_mfma_f32_16x16x32_bf16 v[24:27], v[28:31], v[36:39], v[24:27]\n\t"
    "ds_read_b64_tr_b16 v[36:37], %[r0] offset:22528\n\t"
    "ds_read_b64_tr_b16 v[38:39], %[r1] offset:22528\n\t"
    "ds_read_b128 v[28:31], %[ya] offset:192\n\t"
    "s_waitcnt lgkmcnt(7)\n\t"
    "v_mfma_f32_16x16x32_bf16 v[16:19], v[32:35], v[40:43], v[16:19]\n\t"
    "ds_read_b64_tr_b16 v[40:41], %[r0] offset:23552\n\t"
    "ds_read_b64_tr_b16 v[42:43], %[r1] offset:23552\n\t"
    "v_mfma_f32_16x16x32_bf16 v[20:23], v[32:35], v[44:47], v[20:23]\n\t"
    "ds_read_b64_tr_b16 v[44:45], %[r0] offset:27648\n\t"
    "ds_read_b64_tr_b16 v[46:47], %[r1] offset:27648\n\t"
    "s_waitcnt lgkmcnt(9)\n\t"
    "v_mfma_f32_16x16x32_bf16 v[24:27], v[32:35], v[48:51], v[24:27]\n\t"
    "ds_read_b64_tr_b16 v[48:49], %[r0] offset:28672\n\t"
    "ds_read_b64_tr_b16 v[50:51], %[r1] offset:28672\n\t"
    "ds_read_b128 v[32:35], %[ya] offset:256\n\t"
    "s_waitcnt lgkmcnt(7)\n\t"
    "v_mfma_f32_16x16x32_bf16 v[16:19], v[28:31], v[52:55], v[16:19]\n\t"
    "ds_read_b64_tr_b16 v[52:53], %[r0] offset:29696\n\t"
    "ds_read_b64_tr_b16 v[54:55], %[r1] offset:29696\n\t"
    "v_mfma_f32_16x16x32_bf16 v[20:23], v[28:31], v[36:39], v[20:23]\n\t"
    "s_waitcnt lgkmcnt(7)\n\t"
    "v_mfma_f32_16x16x32_bf16 v[24:27], v[28:31], v[40:43], v[24:27]\n\t"
    "s_waitcnt lgkmcnt(2)\n\t"
    "v_mfma_f32_16x16x32_bf16 v[16:19], v[32:35], v[44:47], v[16:19]\n\t"
    "v_mfma_f32_16x16x32_bf16 v[20:23], v[32:35], v[48:51], v[20:23]\n\t"
    "s_waitcnt lgkmcnt(0)\n\t"
    "v_mfma_f32_16x16x32_bf16 v[24:27], v[32:35], v[52:55], v[24:27]\n\t"
    "s_nop 7\n\t"
    "v_add_f32 %[o1], v16, v17\n\t"
    "v_add_f32 v18, v20, v21\n\t"
    "v_cndmask_b32 %[o1], %[o1], v18, %[g1]\n\t"
    "v_add_f32 v18, v24, v25\n\t"
    "v_cndmask_b32 %[o1], %[o1], v18, %[g2]\n\t"
      : [o0] "=&v"(gp), [o1] "=&v"(up) : [ya] "v"(ya), [r0] "v"(r0), [r1] "v"(r1), [g1] "s"(g1), [g2] "s"(g2) : "memory", TCLOB);
    lds_w32(RED_OFF + 512 * w + 4 * lane, __float_as_uint(gp)); lds_w32(RED_OFF + 512 * w + 256 + 4 * lane, __float_as_uint(up));
    BAR();
    float g = 0.f, u = 0.f;
#pragma unroll
    for (int q = 0; q < 4; ++q) { g += lds_rf(RED_OFF + 512 * q + 4 * lane); u += lds_rf(RED_OFF + 512 * q + 256 + 4 * lane); }
    lds_w32(SG_OFF + 4 * lane, __float_as_uint(g)); lds_w32(SG_OFF + 256 + 4 * lane, __float_as_uint(u));
  }
  STAMP(6);
  {
    LANE();
    int fb;
    if (lane < 32) fb = (((lane & 3) * 8 + 7) << 3) | (lane >> 2);
    else { const int e = lane < 39 ? lane - 32 : 6; fb = (((jj & ~7) | e) << 3) | xg; }
    for (unsigned spins = 0;;) { asm volatile("" ::: "memory");
      const v2u f = __builtin_amdgcn_raw_buffer_load_b64(rs, FLAGS_OFF + fb * 8, 0, SC1);
      if (__all((f.x == (unsigned)ep) & (f.y == (unsigned)(ep >> 32)))) break;
      if (++spins > SPIN_CAP) GIVE_UP();
      __builtin_amdgcn_s_sleep(4);
    }
  }
  STAMP(2);

#pragma unroll 1
  for (int n0 = 0; n0 < 8;) {
    const int nr = 1 + ((TAB_G >> n0) & 1), layer = (TAB_L >> n0) & 1;
    const bool late = n0 > N_LASTQ;
    if (w < nr) {
      LANE();
      const int n = n0 + w;
      const unsigned stag = (TAB_T >> (4 * n)) & 7u;
      const int base = PART_OFF + h * 7 * PART_CU + 512 * (int)((TAB_P >> (4 * n)) & 3u);
      if (!late) {
        for (unsigned spins = 0;;) { asm volatile("" ::: "memory");
          const unsigned t = __builtin_amdgcn_raw_buffer_load_b32(rs, base + (lane < 7 ? lane : 6) * PART_CU + 4, 0, SC1);
          if (__any(((t >> 28) & 7u) == stag)) break;
          if (++spins > SPIN_CAP) GIVE_UP();
          __builtin_amdgcn_s_sleep(2);
        }
        asm volatile("s_barrier" ::: "memory");
      }
      v2u g[7];
#pragma unroll
      for (int q = 0; q < 7; ++q) g[q] = v2u{0u, 0u};
      unsigned miss = 0x7fu;
      for (unsigned spins = 0;;) { asm volatile("" ::: "memory");
#pragma unroll
        for (int q = 0; q < 7; ++q) if (miss & (1u << q)) g[q] = __builtin_amdgcn_raw_buffer_load_b64(rs, base + lane * 8, q * PART_CU, SC1);
#pragma unroll
        for (int q = 0; q < 7; ++q) if ((miss & (1u << q)) && __all(((g[q].y >> 28) & 7u) == stag)) miss &= ~(1u << q);
        if (miss == 0u) break;
        if (++spins > SPIN_CAP) GIVE_UP();
      }
      float lw[7], W = -INFINITY;
#pragma unroll
      for (int q = 0; q < 7; ++q) { lw[q] = __uint_as_float((unsigned)__builtin_amdgcn_ballot_w64((int)g[q].y < 0)); W = fmaxf(W, lw[q]); }
      float a0 = 0.f, a1 = 0.f, a2 = 0.f, L = 0.f;
#pragma unroll
      for (int q = 0; q < 7; ++q) {
        const float f = __builtin_amdgcn_exp2f(lw[q] - W);
        L += f;
        a0 = fmaf(f, __uint_as_float(g[q].x << 12), a0);
        a1 = fmaf(f, __uint_as_float(((g[q].x >> 20) | (g[q].y << 12)) << 12), a1);
        a2 = fmaf(f, __uint_as_float((g[q].y >> 8) << 12), a2);
      }
      const float ri = __builtin_amdgcn_rcpf(L);
      const float ov[3] = {a0 * ri, a1 * ri, a2 * ri};
      const unsigned ob = w == 0 ? OB_OFF : OB2_OFF;
#pragma unroll
      for (int k = 0; k < 3; ++k) {
        const unsigned hi = pk_bf16(ov[k], 0.f), lo = pk_bf16(ov[k] - bf_lo(hi), 0.f);
        lds_w16(ob + 2 * lane + 128 * k, (unsigned short)hi); lds_w16(ob + OB_LO + 2 * lane + 128 * k, (unsigned short)lo);
      }
    } else if (!late) {
      asm volatile("s_barrier" ::: "memory");
      if (w == 1) {
        const int lane = lane_id();
        const __amdgpu_buffer_rsrc_t rd = __builtin_amdgcn_make_buffer_rsrc((void*)(wd + (size_t)h * 48 * 640), 0, 61440, 0x00020000);
        unsigned acc = 0;
#pragma unroll
        for (int k = 0; k < 8; ++k) acc |= __builtin_amdgcn_raw_buffer_load_b32(rd, lane * 128, 8192 * k, SC1);
        asm volatile("" :: "v"(acc));
      }
    }
    BAR();
    STAMP(8 + 6 * n0);
#pragma unroll
    for (int st = 0; st < 2; ++st) {
#pragma unroll 1
      for (int r = 0; r < nr; ++r) {
        const int n = n0 + r;
        const unsigned tl = (n + 1) & 3, th = (n + 1) >> 2;
        float o0, o1, o2;
        if (st == 0) {
          LANE();
          const unsigned ya = (r == 0 ? OB_OFF : OB2_OFF) + OB_LO * (j & 1) + 16 * c;
          unsigned so;
          const unsigned gp = (lane >> 2) * 1280 + (lane & 3) * 16, sb = h * 48 * 1280 + 320 * w;
          if (!late) { asm volatile(
    "ds_read_b128 v[16:19], %[ya] offset:0\n\t"
    "ds_read_b128 v[20:23], %[ya] offset:64\n\t"
    "ds_read_b128 v[24:27], %[ya] offset:128\n\t"
    "ds_read_b128 v[28:31], %[ya] offset:192\n\t"
    "ds_read_b128 v[32:35], %[ya] offset:256\n\t"
    "ds_read_b128 v[36:39], %[ya] offset:320\n\t"
    "s_waitcnt lgkmcnt(0)\n\t"
    "v_mfma_f32_16x16x32_bf16 v[40:43], v[16:19], a[196:199], 0\n\t"
    "v_mfma_f32_16x16x32_bf16 v[40:43], v[20:23], a[204:207], v[40:43]\n\t"
    "v_mfma_f32_16x16x32_bf16 v[40:43], v[24:27], a[212:215], v[40:43]\n\t"
    "v_mfma_f32_16x16x32_bf16 v[40:43], v[28:31], a[220:223], v[40:43]\n\t"
    "v_mfma_f32_16x16x32_bf16 v[40:43], v[32:35], a[228:231], v[40:43]\n\t"
    "v_mfma_f32_16x16x32_bf16 v[40:43], v[36:39], a[236:239], v[40:43]\n\t"
    "v_mfma_f32_16x16x32_bf16 v[44:47], v[16:19], a[200:203], 0\n\t"
    "v_mfma_f32_16x16x32_bf16 v[44:47], v[20:23], a[208:211], v[44:47]\n\t"
    "v_mfma_f32_16x16x32_bf16 v[44:47], v[24:27], a[216:219], v[44:47]\n\t"
    "v_mfma_f32_16x16x32_bf16 v[44:47], v[28:31], a[224:227], v[44:47]\n\t"
    "v_mfma_f32_16x16x32_bf16 v[44:47], v[32:35], a[232:235], v[44:47]\n\t"
    "v_mfma_f32_16x16x32_bf16 v[44:47], v[36:39], a[240:243], v[44:47]\n\t"
    "s_nop 1\n\t"
    "v_add_f32 %[o0], v40, v41\n\t"
    "v_mfma_f32_16x16x32_bf16 v[40:43], v[16:19], a[244:247], 0\n\t"
    "v_mfma_f32_16x16x32_bf16 v[40:43], v[20:23], a[248:251], v[40:43]\n\t"
    "v_mfma_f32_16x16x32_bf16 v[40:43], v[24:27], a[252:255], v[40:43]\n\t"
    "v_mfma_f32_16x16x32_bf16 v[40:43], v[28:31], a[80:83], v[40:43]\n\t"
    "v_mfma_f32_16x16x32_bf16 v[40:43], v[32:35], a[48:51], v[40:43]\n\t"
    "v_mfma_f32_16x16x32_bf16 v[40:43], v[36:39], a[16:19], v[40:43]\n\t"
    "s_nop 1\n\t"
    "v_add_f32 v48, v44, v45\n\t"
    "v_cndmask_b32 %[o0], %[o0], v48, %[g1]\n\t"
    "v_mfma_f32_16x16x32_bf16 v[44:47], v[16:19], a[168:171], 0\n\t"
    "s_add_u32 %[so], %[sb], 0\n\t"
    "buffer_load_dwordx4 a[196:199], %[gp], %[srd], %[so] offen offset:0\n\t"
    "v_mfma_f32_16x16x32_bf16 v[44:47], v[20:23], a[140:143], v[44:47]\n\t"
    "v_mfma_f32_16x16x32_bf16 v[44:47], v[24:27], a[112:115], v[44:47]\n\t"
    "s_add_u32 %[so], %[sb], 20480\n\t"
    "buffer_load_dwordx4 a[200:203], %[gp], %[srd], %[so] offen offset:0\n\t"
    "v_mfma_f32_16x16x32_bf16 v[44:47], v[28:31], a[84:87], v[44:47]\n\t"
    "v_mfma_f32_16x16x32_bf16 v[44:47], v[32:35], a[52:55], v[44:47]\n\t"
    "s_add_u32 %[so], %[sb], 0\n\t"
    "buffer_load_dwordx4 a[204:207], %[gp], %[srd], %[so] offen offset:64\n\t"
    "v_mfma_f32_16x16x32_bf16 v[44:47], v[36:39], a[20:23], v[44:47]\n\t"
    "s_nop 1\n\t"
    "v_add_f32 v48, v40, v41\n\t"
    "v_cndmask_b32 %[o0], %[o0], v48, %[g2]\n\t"
    "v_mfma_f32_16x16x32_bf16 v[40:43], v[16:19], a[172:175], 0\n\t"
    "s_add_u32 %[so], %[sb], 20480\n\t"
    "buffer_load_dwordx4 a[208:211], %[gp], %[srd], %[so] offen offset:64\n\t"
    "v_mfma_f32_16x16x32_bf16 v[40:43], v[20:23], a[144:147], v[40:43]\n\t"
    "v_mfma_f32_16x16x32_bf16 v[40:43], v[24:27], a[116:119], v[40:43]\n\t"
    "s_add_u32 %[so], %[sb], 0\n\t"
    "buffer_load_dwordx4 a[212:215], %[gp], %[srd], %[so] offen offset:128\n\t"
    "v_mfma_f32_16x16x32_bf16 v[40:43], v[28:31], a[88:91], v[40:43]\n\t"
    "v_mfma_f32_16x16x32_bf16 v[40:43], v[32:35], a[56:59], v[40:43]\n\t"
    "s_add_u32 %[so], %[sb], 20480\n\t"
    "buffer_load_dwordx4 a[216:219], %[gp], %[srd], %[so] offen offset:128\n\t"
    "v_mfma_f32_16x16x32_bf16 v[40:43], v[36:39], a[24:27], v[40:43]\n\t"
    "s_nop 1\n\t"
    "v_add_f32 v48, v44, v45\n\t"
    "v_cndmask_b32 %[o0], %[o0], v48, %[g3]\n\t"
    "v_mfma_f32_16x16x32_bf16 v[44:47], v[16:19], a[176:179], 0\n\t"
    "s_add_u32 %[so], %[sb], 0\n\t"
    "buffer_load_dwordx4 a[220:223], %[gp], %[srd], %[so] offen offset:192\n\t"
    "v_mfma_f32_16x16x32_bf16 v[44:47], v[20:23], a[148:151], v[44:47]\n\t"
    "v_mfma_f32_16x16x32_bf16 v[44:47], v[24:27], a[120:123], v[44:47]\n\t"
    "s_add_u32 %[so], %[sb], 20480\n\t"
    "buffer_load_dwordx4 a[224:227], %[gp], %[srd], %[so] offen offset:192\n\t"
    "v_mfma_f32_16x16x32_bf16 v[44:47], v[28:31], a[92:95], v[44:47]\n\t"
    "v_mfma_f32_16x16x32_bf16 v[44:47], v[32:35], a[60:63], v[44:47]\n\t"
    "s_add_u32 %[so], %[sb], 0\n\t"
    "buffer_load_dwordx4 a[228:231], %[gp], %[srd], %[so] offen offset:256\n\t"
    "v_mfma_f32_16x16x32_bf16 v[44:47], v[36:39], a[28:31], v[44:47]\n\t"
    "s_nop 1\n\t"
    "v_add_f32 %[o1], v40, v41\n\t"
    "v_mfma_f32_16x16x32_bf16 v[40:43], v[16:19], a[180:183], 0\n\t"
    "s_add_u32 %[so], %[sb], 20480\n\t"
    "buffer_load_dwordx4 a[232:235], %[gp], %[srd], %[so] offen offset:256\n\t"
    "v_mfma_f32_16x16x32_bf16 v[40:43], v[20:23], a[152:155], v[40:43]\n\t"
    "v_mfma_f32_16x16x32_bf16 v[40:43], v[24:27], a[124:127], v[40:43]\n\t"
    "s_add_u32 %[so], %[sb], 40960\n\t"
    "buffer_load_dwordx4 a[236:239], %[gp], %[srd], %[so] offen offset:0\n\t"
    "v_mfma_f32_16x16x32_bf16 v[40:43], v[28:31], a[96:99], v[40:43]\n\t"
    "v_mfma_f32_16x16x32_bf16 v[40:43], v[32:35], a[64:67], v[40:43]\n\t"
    "s_add_u32 %[so], %[sb], 40960\n\t"
    "buffer_load_dwordx4 a[240:243], %[gp], %[srd], %[so] offen offset:64\n\t"
    "v_mfma_f32_16x16x32_bf16 v[40:43], v[36:39], a[32:35], v[40:43]\n\t"
    "s_nop 1\n\t"
    "v_add_f32 v48, v44, v45\n\t"
    "v_cndmask_b32 %[o1], %[o1], v48, %[g1]\n\t"
    "v_mfma_f32_16x16x32_bf16 v[44:47], v[16:19], a[184:187], 0\n\t"
    "s_add_u32 %[so], %[sb], 40960\n\t"
    "buffer_load_dwordx4 a[244:247], %[gp], %[srd], %[so] offen offset:128\n\t"
    "v_mfma_f32_16x16x32_bf16 v[44:47], v[20:23], a[156:159], v[44:47]\n\t"
    "v_mfma_f32_16x16x32_bf16 v[44:47], v[24:27], a[128:131], v[44:47]\n\t"
    "s_add_u32 %[so], %[sb], 40960\n\t"
    "buffer_load_dwordx4 a[248:251], %[gp], %[srd], %[so] offen offset:192\n\t"
    "v_mfma_f32_16x16x32_bf16 v[44:47], v[28:31], a[100:103], v[44:47]\n\t"
    "v_mfma_f32_16x16x32_bf16 v[44:47], v[32:35], a[68:71], v[44:47]\n\t"
    "s_add_u32 %[so], %[sb], 40960\n\t"
    "buffer_load_dwordx4 a[252:255], %[gp], %[srd], %[so] offen offset:256\n\t"
    "v_mfma_f32_16x16x32_bf16 v[44:47], v[36:39], a[36:39], v[44:47]\n\t"
    "s_nop 1\n\t"
    "v_add_f32 v48, v40, v41\n\t"
    "v_cndmask_b32 %[o1], %[o1], v48, %[g2]\n\t"
    "v_mfma_f32_16x16x32_bf16 v[40:43], v[16:19], a[188:191], 0\n\t"
    "v_mfma_f32_16x16x32_bf16 v[40:43], v[20:23], a[160:163], v[40:43]\n\t"
    "v_mfma_f32_16x16x32_bf16 v[40:43], v[24:27], a[132:135], v[40:43]\n\t"
    "v_mfma_f32_16x16x32_bf16 v[40:43], v[28:31], a[104:107], v[40:43]\n\t"
    "v_mfma_f32_16x16x32_bf16 v[40:43], v[32:35], a[72:75], v[40:43]\n\t"
    "v_mfma_f32_16x16x32_bf16 v[40:43], v[36:39], a[40:43], v[40:43]\n\t"
    "s_nop 1\n\t"
    "v_add_f32 v48, v44, v45\n\t"
    "v_cndmask_b32 %[o1], %[o1], v48, %[g3]\n\t"
    "v_mfma_f32_16x16x32_bf16 v[44:47], v[16:19], a[192:195], 0\n\t"
    "v_mfma_f32_16x16x32_bf16 v[44:47], v[20:23], a[164:167], v[44:47]\n\t"
    "v_mfma_f32_16x16x32_bf16 v[44:47], v[24:27], a[136:139], v[44:47]\n\t"
    "v_mfma_f32_16x16x32_bf16 v[44:47], v[28:31], a[108:111], v[44:47]\n\t"
    "v_mfma_f32_16x16x32_bf16 v[44:47], v[32:35], a[76:79], v[44:47]\n\t"
    "v_mfma_f32_16x16x32_bf16 v[44:47], v[36:39], a[44:47], v[44:47]\n\t"
    "s_nop 1\n\t"
    "v_add_f32 %[o2], v40, v41\n\t"
    "s_nop 7\n\t"
    "v_add_f32 v48, v44, v45\n\t"
    "v_cndmask_b32 %[o2], %[o2], v48, %[g1]\n\t"
            : [o0] "=&v"(o0), [o1] "=&v"(o1), [o2] "=&v"(o2), [so] "=&s"(so)
            : [ya] "v"(ya), [g1] "s"(g1), [g2] "s"(g2), [g3] "s"(g3), [gp] "v"(gp), [srd] "s"(srd_wd), [sb] "s"(sb) : "memory", "scc", TCLOB); }
          else { asm volatile(
    "ds_read_b128 v[16:19], %[ya] offset:0\n\t"
    "ds_read_b128 v[20:23], %[ya] offset:64\n\t"
    "ds_read_b128 v[24:27], %[ya] offset:128\n\t"
    "ds_read_b128 v[28:31], %[ya] offset:192\n\t"
    "ds_read_b128 v[32:35], %[ya] offset:256\n\t"
    "ds_read_b128 v[36:39], %[ya] offset:320\n\t"
    "s_waitcnt lgkmcnt(0)\n\t"
    "v_mfma_f32_16x16x32_bf16 v[40:43], v[16:19], v[56:59], 0\n\t"
    "v_mfma_f32_16x16x32_bf16 v[40:43], v[20:23], v[64:67], v[40:43]\n\t"
    "v_mfma_f32_16x16x32_bf16 v[40:43], v[24:27], v[72:75], v[40:43]\n\t"
    "v_mfma_f32_16x16x32_bf16 v[40:43], v[28:31], v[80:83], v[40:43]\n\t"
    "v_mfma_f32_16x16x32_bf16 v[40:43], v[32:35], v[88:91], v[40:43]\n\t"
    "v_mfma_f32_16x16x32_bf16 v[40:43], v[36:39], v[96:99], v[40:43]\n\t"
    "v_mfma_f32_16x16x32_bf16 v[44:47], v[16:19], v[60:63], 0\n\t"
    "v_mfma_f32_16x16x32_bf16 v[44:47], v[20:23], v[68:71], v[44:47]\n\t"
    "v_mfma_f32_16x16x32_bf16 v[44:47], v[24:27], v[76:79], v[44:47]\n\t"
    "v_mfma_f32_16x16x32_bf16 v[44:47], v[28:31], v[84:87], v[44:47]\n\t"
    "v_mfma_f32_16x16x32_bf16 v[44:47], v[32:35], v[92:95], v[44:47]\n\t"
    "v_mfma_f32_16x16x32_bf16 v[44:47], v[36:39], v[100:103], v[44:47]\n\t"
    "s_nop 1\n\t"
    "v_add_f32 %[o0], v40, v41\n\t"
    "v_mfma_f32_16x16x32_bf16 v[40:43], v[16:19], v[104:107], 0\n\t"
    "v_mfma_f32_16x16x32_bf16 v[40:43], v[20:23], v[108:111], v[40:43]\n\t"
    "v_mfma_f32_16x16x32_bf16 v[40:43], v[24:27], v[112:115], v[40:43]\n\t"
    "v_mfma_f32_16x16x32_bf16 v[40:43], v[28:31], a[80:83], v[40:43]\n\t"
    "v_mfma_f32_16x16x32_bf16 v[40:43], v[32:35], a[48:51], v[40:43]\n\t"
    "v_mfma_f32_16x16x32_bf16 v[40:43], v[36:39], a[16:19], v[40:43]\n\t"
    "s_nop 1\n\t"
    "v_add_f32 v48, v44, v45\n\t"
    "v_cndmask_b32 %[o0], %[o0], v48, %[g1]\n\t"
    "v_mfma_f32_16x16x32_bf16 v[44:47], v[16:19], a[168:171], 0\n\t"
    "v_mfma_f32_16x16x32_bf16 v[44:47], v[20:23], a[140:143], v[44:47]\n\t"
    "v_mfma_f32_16x16x32_bf16 v[44:47], v[24:27], a[112:115], v[44:47]\n\t"
    "v_mfma_f32_16x16x32_bf16 v[44:47], v[28:31], a[84:87], v[44:47]\n\t"
    "v_mfma_f32_16x16x32_bf16 v[44:47], v[32:35], a[52:55], v[44:47]\n\t"
    "v_mfma_f32_16x16x32_bf16 v[44:47], v[36:39], a[20:23], v[44:47]\n\t"
    "s_nop 1\n\t"
    "v_add_f32 v48, v40, v41\n\t"
    "v_cndmask_b32 %[o0], %[o0], v48, %[g2]\n\t"
    "v_mfma_f32_16x16x32_bf16 v[40:43], v[16:19], a[172:175], 0\n\t"
    "v_mfma_f32_16x16x32_bf16 v[40:43], v[20:23], a[144:147], v[40:43]\n\t"
    "v_mfma_f32_16x16x32_bf16 v[40:43], v[24:27], a[116:119], v[40:43]\n\t"
    "v_mfma_f32_16x16x32_bf16 v[40:43], v[28:31], a[88:91], v[40:43]\n\t"
    "v_mfma_f32_16x16x32_bf16 v[40:43], v[32:35], a[56:59], v[40:43]\n\t"
    "v_mfma_f32_16x16x32_bf16 v[40:43], v[36:39], a[24:27], v[40:43]\n\t"
    "s_nop 1\n\t"
    "v_add_f32 v48, v44, v45\n\t"
    "v_cndmask_b32 %[o0], %[o0], v48, %[g3]\n\t"
    "v_mfma_f32_16x16x32_bf16 v[44:47], v[16:19], a[176:179], 0\n\t"
    "v_mfma_f32_16x16x32_bf16 v[44:47], v[20:23], a[148:151], v[44:47]\n\t"
    "v_mfma_f32_16x16x32_bf16 v[44:47], v[24:27], a[120:123], v[44:47]\n\t"
    "v_mfma_f32_16x16x32_bf16 v[44:47], v[28:31], a[92:95], v[44:47]\n\t"
    "v_mfma_f32_16x16x32_bf16 v[44:47], v[32:35], a[60:63], v[44:47]\n\t"
    "v_mfma_f32_16x16x32_bf16 v[44:47], v[36:39], a[28:31], v[44:47]\n\t"
    "s_nop 1\n\t"
    "v_add_f32 %[o1], v40, v41\n\t"
    "v_mfma_f32_16x16x32_bf16 v[40:43], v[16:19], a[180:183], 0\n\t"
    "v_mfma_f32_16x16x32_bf16 v[40:43], v[20:23], a[152:155], v[40:43]\n\t"
    "v_mfma_f32_16x16x32_bf16 v[40:43], v[24:27], a[124:127], v[40:43]\n\t"
    "v_mfma_f32_16x16x32_bf16 v[40:43], v[28:31], a[96:99], v[40:43]\n\t"
    "v_mfma_f32_16x16x32_bf16 v[40:43], v[32:35], a[64:67], v[40:43]\n\t"
    "v_mfma_f32_16x16x32_bf16 v[40:43], v[36:39], a[32:35], v[40:43]\n\t"
    "s_nop 1\n\t"
    "v_add_f32 v48, v44, v45\n\t"
    "v_cndmask_b32 %[o1], %[o1], v48, %[g1]\n\t"
    "v_mfma_f32_16x16x32_bf16 v[44:47], v[16:19], a[184:187], 0\n\t"
    "v_mfma_f32_16x16x32_bf16 v[44:47], v[20:23], a[156:159], v[44:47]\n\t"
    "v_mfma_f32_16x16x32_bf16 v[44:47], v[24:27], a[128:131], v[44:47]\n\t"
    "v_mfma_f32_16x16x32_bf16 v[44:47], v[28:31], a[100:103], v[44:47]\n\t"
    "v_mfma_f32_16x16x32_bf16 v[44:47], v[32:35], a[68:71], v[44:47]\n\t"
    "v_mfma_f32_16x16x32_bf16 v[44:47], v[36:39], a[36:39], v[44:47]\n\t"
    "s_nop 1\n\t"
    "v_add_f32 v48, v40, v41\n\t"
    "v_cndmask_b32 %[o1], %[o1], v48, %[g2]\n\t"
    "v_mfma_f32_16x16x32_bf16 v[40:43], v[16:19], a[188:191], 0\n\t"
    "v_mfma_f32_16x16x32_bf16 v[40:43], v[20:23], a[160:163], v[40:43]\n\t"
    "v_mfma_f32_16x16x32_bf16 v[40:43], v[24:27], a[132:135], v[40:43]\n\t"
    "v_mfma_f32_16x16x32_bf16 v[40:43], v[28:31], a[104:107], v[40:43]\n\t"
    "v_mfma_f32_16x16x32_bf16 v[40:43], v[32:35], a[72:75], v[40:43]\n\t"
    "v_mfma_f32_16x16x32_bf16 v[40:43], v[36:39], a[40:43], v[40:43]\n\t"
    "s_nop 1\n\t"
    "v_add_f32 v48, v44, v45\n\t"
    "v_cndmask_b32 %[o1], %[o1], v48, %[g3]\n\t"
    "v_mfma_f32_16x16x32_bf16 v[44:47], v[16:19], a[192:195], 0\n\t"
    "v_mfma_f32_16x16x32_bf16 v[44:47], v[20:23], a[164:167], v[44:47]\n\t"
    "v_mfma_f32_16x16x32_bf16 v[44:47], v[24:27], a[136:139], v[44:47]\n\t"
    "v_mfma_f32_16x16x32_bf16 v[44:47], v[28:31], a[108:111], v[44:47]\n\t"
    "v_mfma_f32_16x16x32_bf16 v[44:47], v[32:35], a[76:79], v[44:47]\n\t"
    "v_mfma_f32_16x16x32_bf16 v[44:47], v[36:39], a[44:47], v[44:47]\n\t"
    "s_nop 1\n\t"
    "v_add_f32 %[o2], v40, v41\n\t"
    "s_nop 7\n\t"
    "v_add_f32 v48, v44, v45\n\t"
    "v_cndmask_b32 %[o2], %[o2], v48, %[g1]\n\t"
            : [o0] "=&v"(o0), [o1] "=&v"(o1), [o2] "=&v"(o2)
            : [ya] "v"(ya), [g1] "s"(g1), [g2] "s"(g2), [g3] "s"(g3) : "memory", TCLOB); }
        } else {
          const unsigned red = r == 0 ? RED_OFF : RED2_OFF;
          gather_quarter(rs, X_OFF + r * 5120, tl, th, w, red + 512 * w + 192, red + 512 * w + 256 + 192);
          STAMP(10 + 6 * n);
          float gp, up;
          { LANE();
            const unsigned sw = (c & 1) << 2;
            const unsigned r0 = stage + ((8 * c + (j >> 2)) ^ sw) * 32 + 8 * (j & 3), r1 = stage + ((8 * c + 4 + (j >> 2)) ^ sw) * 32 + 8 * (j & 3);
            const unsigned ya = YS_OFF + 1280 * (j & 1) + 16 * c + 320 * w;
            asm volatile(
    "ds_read_b128 v[28:31], %[ya] offset:0\n\t"
    "ds_read_b64_tr_b16 v[36:37], %[r0] offset:0\n\t"
    "ds_read_b64_tr_b16 v[38:39], %[r1] offset:0\n\t"
    "ds_read_b64_tr_b16 v[40:41], %[r0] offset:1024\n\t"
    "ds_read_b64_tr_b16 v[42:43], %[r1] offset:1024\n\t"
    "ds_read_b64_tr_b16 v[44:45], %[r0] offset:2048\n\t"
    "ds_read_b64_tr_b16 v[46:47], %[r1] offset:2048\n\t"
    "ds_read_b128 v[32:35], %[ya] offset:64\n\t"
    "ds_read_b64_tr_b16 v[48:49], %[r0] offset:6144\n\t"
    "ds_read_b64_tr_b16 v[50:51], %[r1] offset:6144\n\t"
    "ds_read_b64_tr_b16 v[52:53], %[r0] offset:7168\n\t"
    "ds_read_b64_tr_b16 v[54:55], %[r1] offset:7168\n\t"
    "s_waitcnt lgkmcnt(9)\n\t"
    "v_mfma_f32_16x16x32_bf16 v[16:19], v[28:31], v[36:39], 0\n\t"
    "ds_read_b64_tr_b16 v[36:37], %[r0] offset:8192\n\t"
    "ds_read_b64_tr_b16 v[38:39], %[r1] offset:8192\n\t"
    "s_waitcnt lgkmcnt(9)\n\t"
    "v_mfma_f32_16x16x32_bf16 v[20:23], v[28:31], v[40:43], 0\n\t"
    "ds_read_b64_tr_b16 v[40:41], %[r0] offset:12288\n\t"
    "ds_read_b64_tr_b16 v[42:43], %[r1] offset:12288\n\t"
    "s_waitcnt lgkmcnt(9)\n\t"
    "v_mfma_f32_16x16x32_bf16 v[24:27], v[28:31], v[44:47], 0\n\t"
    "ds_read_b64_tr_b16 v[44:45], %[r0] offset:13312\n\t"
    "ds_read_b64_tr_b16 v[46:47], %[r1] offset:13312\n\t"
    "ds_read_b128 v[28:31], %[ya] offset:128\n\t"
    "s_waitcnt lgkmcnt(9)\n\t"
    "v_mfma_f32_16x16x32_bf16 v[16:19], v[32:35], v[48:51], v[16:19]\n\t"
    "ds_read_b64_tr_b16 v[48:49], %[r0] offset:14336\n\t"
    "ds_read_b64_tr_b16 v[50:51], %[r1] offset:14336\n\t"
    "s_waitcnt lgkmcnt(9)\n\t"
    "v_mfma_f32_16x16x32_bf16 v[20:23], v[32:35], v[52:55], v[20:23]\n\t"
    "ds_read_b64_tr_b16 v[52:53], %[r0] offset:18432\n\t"
    "ds_read_b64_tr_b16 v[54:55], %[r1] offset:18432\n\t"
    "s_waitcnt lgkmcnt(9)\n\t"
    "v_mfma_f32_16x16x32_bf16 v[24:27], v[32:35], v[36:39], v[24:27]\n\t"
    "ds_read_b64_tr_b16 v[36:37], %[r0] offset:19456\n\t"
    "ds_read_b64_tr_b16 v[38:39], %[r1] offset:19456\n\t"
    "ds_read_b128 v[32:35], %[ya] offset:192\n\t"
    "s_waitcnt lgkmcnt(7)\n\t"
    "v_mfma_f32_16x16x32_bf16 v[16:19], v[28:31], v[40:43], v[16:19]\n\t"
    "ds_read_b64_tr_b16 v[40:41], %[r0] offset:20480\n\t"
    "ds_read_b64_tr_b16 v[42:43], %[r1] offset:20480\n\t"
    "v_mfma_f32_16x16x32_bf16 v[20:23], v[28:31], v[44:47], v[20:23]\n\t"
    "ds_read_b64_tr_b16 v[44:45], %[r0] offset:24576\n\t"
    "ds_read_b64_tr_b16 v[46:47], %[r1] offset:24576\n\t"
    "s_waitcnt lgkmcnt(9)\n\t"
    "v_mfma_f32_16x16x32_bf16 v[24:27], v[28:31], v[48:51], v[24:27]\n\t"
    "ds_read_b64_tr_b16 v[48:49], %[r0] offset:25600\n\t"
    "ds_read_b64_tr_b16 v[50:51], %[r1] offset:25600\n\t"
    "ds_read_b128 v[28:31], %[ya] offset:256\n\t"
    "s_waitcnt lgkmcnt(7)\n\t"
    "v_mfma_f32_16x16x32_bf16 v[16:19], v[32:35], v[52:55], v[16:19]\n\t"
    "ds_read_b64_tr_b16 v[52:53], %[r0] offset:26624\n\t"
    "ds_read_b64_tr_b16 v[54:55], %[r1] offset:26624\n\t"
    "v_mfma_f32_16x16x32_bf16 v[20:23], v[32:35], v[36:39], v[20:23]\n\t"
    "ds_read_b64_tr_b16 v[36:37], %[r0] offset:3072\n\t"
    "ds_read_b64_tr_b16 v[38:39], %[r1] offset:3072\n\t"
    "s_waitcnt lgkmcnt(9)\n\t"
    "v_mfma_f32_16x16x32_bf16 v[24:27], v[32:35], v[40:43], v[24:27]\n\t"
    "ds_read_b64_tr_b16 v[40:41], %[r0] offset:4096\n\t"
    "ds_read_b64_tr_b16 v[42:43], %[r1] offset:4096\n\t"
    "ds_read_b128 v[32:35], %[ya] offset:0\n\t"
    "s_waitcnt lgkmcnt(7)\n\t"
    "v_mfma_f32_16x16x32_bf16 v[16:19], v[28:31], v[44:47], v[16:19]\n\t"
    "ds_read_b64_tr_b16 v[44:45], %[r0] offset:5120\n\t"
    "ds_read_b64_tr_b16 v[46:47], %[r1] offset:5120\n\t"
    "v_mfma_f32_16x16x32_bf16 v[20:23], v[28:31], v[48:51], v[20:23]\n\t"
    "ds_read_b64_tr_b16 v[48:49], %[r0] offset:9216\n\t"
    "ds_read_b64_tr_b16 v[50:51], %[r1] offset:9216\n\t"
    "s_waitcnt lgkmcnt(9)\n\t"
    "v_mfma_f32_16x16x32_bf16 v[24:27], v[28:31], v[52:55], v[24:27]\n\t"
    "ds_read_b64_tr_b16 v[52:53], %[r0] offset:10240\n\t"
    "ds_read_b64_tr_b16 v[54:55], %[r1] offset:10240\n\t"
    "ds_read_b128 v[28:31], %[ya] offset:64\n\t"
    "s_nop 7\n\t"
    "v_add_f32 %[o0], v16, v17\n\t"
    "v_add_f32 v18, v20, v21\n\t"
    "v_cndmask_b32 %[o0], %[o0], v18, %[g1]\n\t"
    "v_add_f32 v18, v24, v25\n\t"
    "v_cndmask_b32 %[o0], %[o0], v18, %[g2]\n\t"
    "s_waitcnt lgkmcnt(7)\n\t"
    "v_mfma_f32_16x16x32_bf16 v[16:19], v[32:35], v[36:39], 0\n\t"
    "ds_read_b64_tr_b16 v[36:37], %[r0] offset:11264\n\t"
    "ds_read_b64_tr_b16 v[38:39], %[r1] offset:11264\n\t"
    "v_mfma_f32_16x16x32_bf16 v[20:23], v[32:35], v[40:43], 0\n\t"
    "ds_read_b64_tr_b16 v[40:41], %[r0] offset:15360\n\t"
    "ds_read_b64_tr_b16 v[42:43], %[r1] offset:15360\n\t"
    "s_waitcnt lgkmcnt(9)\n\t"
    "v_mfma_f32_16x16x32_bf16 v[24:27], v[32:35], v[44:47], 0\n\t"
    "ds_read_b64_tr_b16 v[44:45], %[r0] offset:16384\n\t"
    "ds_read_b64_tr_b16 v[46:47], %[r1] offset:16384\n\t"
    "ds_read_b128 v[32:35], %[ya] offset:128\n\t"
    "s_waitcnt lgkmcnt(7)\n\t"
    "v_mfma_f32_16x16x32_bf16 v[16:19], v[28:31], v[48:51], v[16:19]\n\t"
    "ds_read_b64_tr_b16 v[48:49], %[r0] offset:17408\n\t"
    "ds_read_b64_tr_b16 v[50:51], %[r1] offset:17408\n\t"
    "v_mfma_f32_16x16x32_bf16 v[20:23], v[28:31], v[52:55], v[20:23]\n\t"
    "ds_read_b64_tr_b16 v[52:53], %[r0] offset:21504\n\t"
    "ds_read_b64_tr_b16 v[54:55], %[r1] offset:21504\n\t"
    "s_waitcnt lgkmcnt(9)\n\t"
    "v_mfma_f32_16x16x32_bf16 v[24:27], v[28:31], v[36:39], v[24:27]\n\t"
    "ds_read_b64_tr_b16 v[36:37], %[r0] offset:22528\n\t"
    "ds_read_b64_tr_b16 v[38:39], %[r1] offset:22528\n\t"
    "ds_read_b128 v[28:31], %[ya] offset:192\n\t"
    "s_waitcnt lgkmcnt(7)\n\t"
    "v_mfma_f32_16x16x32_bf16 v[16:19], v[32:35], v[40:43], v[16:19]\n\t"
    "ds_read_b64_tr_b16 v[40:41], %[r0] offset:23552\n\t"
    "ds_read_b64_tr_b16 v[42:43], %[r1] offset:23552\n\t"
    "v_mfma_f32_16x16x32_bf16 v[20:23], v[32:35], v[44:47], v[20:23]\n\t"
    "ds_read_b64_tr_b16 v[44:45], %[r0] offset:27648\n\t"
    "ds_read_b64_tr_b16 v[46:47], %[r1] offset:27648\n\t"
    "s_waitcnt lgkmcnt(9)\n\t"
    "v_mfma_f32_16x16x32_bf16 v[24:27], v[32:35], v[48:51], v[24:27]\n\t"
    "ds_read_b64_tr_b16 v[48:49], %[r0] offset:28672\n\t"
    "ds_read_b64_tr_b16 v[50:51], %[r1] offset:28672\n\t"
    "ds_read_b128 v[32:35], %[ya] offset:256\n\t"
    "s_waitcnt lgkmcnt(7)\n\t"
    "v_mfma_f32_16x16x32_bf16 v[16:19], v[28:31], v[52:55], v[16:19]\n\t"
    "ds_read_b64_tr_b16 v[52:53], %[r0] offset:29696\n\t"
    "ds_read_b64_tr_b16 v[54:55], %[r1] offset:29696\n\t"
    "v_mfma_f32_16x16x32_bf16 v[20:23], v[28:31], v[36:39], v[20:23]\n\t"
    "s_waitcnt lgkmcnt(7)\n\t"
    "v_mfma_f32_16x16x32_bf16 v[24:27], v[28:31], v[40:43], v[24:27]\n\t"
    "s_waitcnt lgkmcnt(2)\n\t"
    "v_mfma_f32_16x16x32_bf16 v[16:19], v[32:35], v[44:47], v[16:19]\n\t"
    "v_mfma_f32_16x16x32_bf16 v[20:23], v[32:35], v[48:51], v[20:23]\n\t"
    "s_waitcnt lgkmcnt(0)\n\t"
    "v_mfma_f32_16x16x32_bf16 v[24:27], v[32:35], v[52:55], v[24:27]\n\t"
    "s_nop 7\n\t"
    "v_add_f32 %[o1], v16, v17\n\t"
    "v_add_f32 v18, v20, v21\n\t"
    "v_cndmask_b32 %[o1], %[o1], v18, %[g1]\n\t"
    "v_add_f32 v18, v24, v25\n\t"
    "v_cndmask_b32 %[o1], %[o1], v18, %[g2]\n\t"
            : [o0] "=&v"(gp), [o1] "=&v"(up) : [ya] "v"(ya), [r0] "v"(r0), [r1] "v"(r1), [g1] "s"(g1), [g2] "s"(g2) : "memory", TCLOB);
          }
          { LANE();
            if (lane < 48) { lds_w32(red + 512 * w + 4 * lane, __float_as_uint(gp)); lds_w32(red + 512 * w + 256 + 4 * lane, __float_as_uint(up)); }
            const float sg = lds_rf(SG_OFF + 4 * lane), su = lds_rf(SG_OFF + 256 + 4 * lane);
            BAR();
            float g = 0.f, u = 0.f;
#pragma unroll
            for (int q = 0; q < 4; ++q) { g += lds_rf(red + 512 * q + 4 * lane); u += lds_rf(red + 512 * q + 256 + 4 * lane); }
            const float mean = rdl(g, 48) * (1.0f / 640.0f);
            const float rstd = __builtin_amdgcn_rsqf(fmaxf(fmaf(-mean, mean, rdl(u, 48) * (1.0f / 640.0f)), 0.f) + 1e-5f);
            g = (g - mean * sg) * rstd; u = (u - mean * su) * rstd;
            const float z = 0.7978845608028654f * (g + 0.044715f * g * g * g);
            const float tn = 1.0f - 2.0f * __builtin_amdgcn_rcpf(__builtin_amdgcn_exp2f(2.8853900817779268f * z) + 1.0f);
            const float hv = 0.5f * g * (1.0f + tn) * u;
            const unsigned hi = pk_bf16(hv, 0.f), lo = pk_bf16(hv - bf_lo(hi), 0.f);
            lds_w16(HB_OFF + 256 * w + 2 * lane, (unsigned short)hi); lds_w16(HB_OFF + 256 * w + 128 + 2 * lane, (unsigned short)lo);
            const unsigned yc = HB_OFF + 256 * w + 128 * (j & 1) + 16 * c, yb = HB_OFF + 256 * w + 128 * (j & 1) + 64 + 8 * c;
            asm volatile(
    "ds_read_b128 v[16:19], %[ya]\n\t"
    "ds_read_b64 v[20:21], %[yb]\n\t"
    "s_waitcnt lgkmcnt(0)\n\t"
    "v_mfma_f32_16x16x32_bf16 v[24:27], v[16:19], a[196:199], 0\n\t"
    "v_mfma_f32_16x16x16_bf16 v[28:31], v[20:21], a[236:237], 0\n\t"
    "v_mfma_f32_16x16x32_bf16 v[32:35], v[16:19], a[200:203], 0\n\t"
    "v_mfma_f32_16x16x16_bf16 v[36:39], v[20:21], a[238:239], 0\n\t"
    "v_mfma_f32_16x16x32_bf16 v[40:43], v[16:19], a[204:207], 0\n\t"
    "v_mfma_f32_16x16x16_bf16 v[44:47], v[20:21], a[240:241], 0\n\t"
    "v_mfma_f32_16x16x32_bf16 v[48:51], v[16:19], a[208:211], 0\n\t"
    "v_mfma_f32_16x16x16_bf16 v[52:55], v[20:21], a[242:243], 0\n\t"
    "s_nop 7\n\t"
    "v_add_f32 v22, v24, v25\n\t"
    "v_add_f32 v23, v28, v29\n\t"
    "v_add_f32 %[o0], v22, v23\n\t"
    "v_add_f32 v22, v32, v33\n\t"
    "v_add_f32 v23, v36, v37\n\t"
    "v_add_f32 v22, v22, v23\n\t"
    "v_cndmask_b32 %[o0], %[o0], v22, %[g1]\n\t"
    "v_add_f32 v22, v40, v41\n\t"
    "v_add_f32 v23, v44, v45\n\t"
    "v_add_f32 v22, v22, v23\n\t"
    "v_cndmask_b32 %[o0], %[o0], v22, %[g2]\n\t"
    "v_add_f32 v22, v48, v49\n\t"
    "v_add_f32 v23, v52, v53\n\t"
    "v_add_f32 v22, v22, v23\n\t"
    "v_cndmask_b32 %[o0], %[o0], v22, %[g3]\n\t"
    "v_mfma_f32_16x16x32_bf16 v[24:27], v[16:19], a[212:215], 0\n\t"
    "v_mfma_f32_16x16x16_bf16 v[28:31], v[20:21], a[244:245], 0\n\t"
    "v_mfma_f32_16x16x32_bf16 v[32:35], v[16:19], a[216:219], 0\n\t"
    "v_mfma_f32_16x16x16_bf16 v[36:39], v[20:21], a[246:247], 0\n\t"
    "v_mfma_f32_16x16x32_bf16 v[40:43], v[16:19], a[220:223], 0\n\t"
    "v_mfma_f32_16x16x16_bf16 v[44:47], v[20:21], a[248:249], 0\n\t"
    "v_mfma_f32_16x16x32_bf16 v[48:51], v[16:19], a[224:227], 0\n\t"
    "v_mfma_f32_16x16x16_bf16 v[52:55], v[20:21], a[250:251], 0\n\t"
    "s_nop 7\n\t"
    "v_add_f32 v22, v24, v25\n\t"
    "v_add_f32 v23, v28, v29\n\t"
    "v_add_f32 %[o1], v22, v23\n\t"
    "v_add_f32 v22, v32, v33\n\t"
    "v_add_f32 v23, v36, v37\n\t"
    "v_add_f32 v22, v22, v23\n\t"
    "v_cndmask_b32 %[o1], %[o1], v22, %[g1]\n\t"
    "v_add_f32 v22, v40, v41\n\t"
    "v_add_f32 v23, v44, v45\n\t"
    "v_add_f32 v22, v22, v23\n\t"
    "v_cndmask_b32 %[o1], %[o1], v22, %[g2]\n\t"
    "v_add_f32 v22, v48, v49\n\t"
    "v_add_f32 v23, v52, v53\n\t"
    "v_add_f32 v22, v22, v23\n\t"
    "v_cndmask_b32 %[o1], %[o1], v22, %[g3]\n\t"
    "v_mfma_f32_16x16x32_bf16 v[24:27], v[16:19], a[228:231], 0\n\t"
    "v_mfma_f32_16x16x16_bf16 v[28:31], v[20:21], a[252:253], 0\n\t"
    "v_mfma_f32_16x16x32_bf16 v[32:35], v[16:19], a[232:235], 0\n\t"
    "v_mfma_f32_16x16x16_bf16 v[36:39], v[20:21], a[254:255], 0\n\t"
    "s_nop 7\n\t"
    "v_add_f32 v22, v24, v25\n\t"
    "v_add_f32 v23, v28, v29\n\t"
    "v_add_f32 %[o2], v22, v23\n\t"
    "v_add_f32 v22, v32, v33\n\t"
    "v_add_f32 v23, v36, v37\n\t"
    "v_add_f32 v22, v22, v23\n\t"
    "v_cndmask_b32 %[o2], %[o2], v22, %[g1]\n\t"
            : [o0] "=&v"(o0), [o1] "=&v"(o1), [o2] "=&v"(o2) : [ya] "v"(yc), [yb] "v"(yb), [g1] "s"(g1), [g2] "s"(g2), [g3] "s"(g3) : "memory", TCLOB); }
        }
        { LANE();
          const float n0_ = dppf<0xB1>(o0), n1 = dppf<0xB1>(o1), n2 = dppf<0xB1>(o2);
          const bool odd = j & 1;
          const int pst = P_OFF + (2 * r + st) * 81920 + h * 2560 + w * 640 + c * 64 + (j >> 1) * 8;
          __builtin_amdgcn_raw_buffer_store_b64(v2u{(__float_as_uint(odd ? n1 : o0) & ~3u) | tl, (__float_as_uint(odd ? o1 : n0_) & ~3u) | th}, rs, pst + (odd ? 256 : 0), 0, SC1);
          if (c < 2 && !odd) __builtin_amdgcn_raw_buffer_store_b64(v2u{(__float_as_uint(o2) & ~3u) | tl, (__float_as_uint(n2) & ~3u) | th}, rs, pst + 512, 0, SC1);
        }
        STAMP(9 + 2 * st + 6 * n);
      }
      if (rho < 80) {
#pragma unroll 1
        for (int r = 0; r < nr; ++r) {
          LANE();
          const int n = n0 + r, b = (TAB_B >> (4 * n)) & 3;
          const unsigned tl = (n + 1) & 3, th = (n + 1) >> 2;
          v4u t;
          for (unsigned spins = 0;;) { asm volatile("" ::: "memory");
            t = __builtin_amdgcn_raw_buffer_load_b128(rs, P_OFF + (2 * r + st) * 81920 + (lane >> 1) * 2560 + rho * 32 + (lane & 1) * 16, 0, SC1);
            if (__all(((t.x & 3u) == tl) & ((t.y & 3u) == th) & ((t.z & 3u) == tl) & ((t.w & 3u) == th))) break;
            if (++spins > SPIN_CAP) GIVE_UP();
          }
          float a[4] = {__uint_as_float(t.x & ~3u), __uint_as_float(t.y & ~3u), __uint_as_float(t.z & ~3u), __uint_as_float(t.w & ~3u)};
          const unsigned ad = RES_OFF + (b * 3 + w) * 32 + (lane & 1) * 16;
#pragma unroll
          for (int k = 0; k < 4; ++k) { a[k] += dppf<0x4E>(a[k]); a[k] += dppf<0x124>(a[k]); a[k] += dppf<0x128>(a[k]); a[k] = xrow_sum(a[k]) + lds_rf(ad + 4 * k); }
          if (lane < 2) {
            if (st == 1 && layer == 1) *(v2u*)(out + b * 640 + 8 * rho + 4 * lane) = v2u{pk_bf16(a[0], a[1]), pk_bf16(a[2], a[3])};
            else __builtin_amdgcn_raw_buffer_store_b128(v4u{(__float_as_uint(a[0]) & ~3u) | tl, (__float_as_uint(a[1]) & ~3u) | th, (__float_as_uint(a[2]) & ~3u) | tl, (__float_as_uint(a[3]) & ~3u) | th},
                                                        rs, X_OFF + r * 5120 + st * 2560 + rho * 32 + lane * 16, 0, SC1);
#pragma unroll
            for (int k = 0; k < 4; ++k) lds_w32(ad + 4 * k, __float_as_uint(a[k]));
          }
        }
      }
      if (st == 0 && !late) {
        LANE();
        const unsigned wa = xs + 16 * lane, rp = xs + (8 * c + (j >> 2)) * 64 + 8 * (j & 3), rq = xs + (4 * c + (j >> 2)) * 64 + 8 * (j & 3);
        asm volatile(
    "s_waitcnt vmcnt(2)\n\t"
    "ds_write_b128 %[wa], a[196:199] offset:0\n\t"
    "ds_write_b128 %[wa], a[200:203] offset:1024\n\t"
    "ds_read_b64_tr_b16 a[196:197], %[rp] offset:0\n\t"
    "ds_read_b64_tr_b16 a[198:199], %[rp] offset:256\n\t"
    "ds_read_b64_tr_b16 a[200:201], %[rp] offset:32\n\t"
    "ds_read_b64_tr_b16 a[202:203], %[rp] offset:288\n\t"
    "ds_write_b128 %[wa], a[204:207] offset:0\n\t"
    "ds_write_b128 %[wa], a[208:211] offset:1024\n\t"
    "ds_read_b64_tr_b16 a[204:205], %[rp] offset:0\n\t"
    "ds_read_b64_tr_b16 a[206:207], %[rp] offset:256\n\t"
    "ds_read_b64_tr_b16 a[208:209], %[rp] offset:32\n\t"
    "ds_read_b64_tr_b16 a[210:211], %[rp] offset:288\n\t"
    "ds_write_b128 %[wa], a[212:215] offset:0\n\t"
    "ds_write_b128 %[wa], a[216:219] offset:1024\n\t"
    "ds_read_b64_tr_b16 a[212:213], %[rp] offset:0\n\t"
    "ds_read_b64_tr_b16 a[214:215], %[rp] offset:256\n\t"
    "ds_read_b64_tr_b16 a[216:217], %[rp] offset:32\n\t"
    "ds_read_b64_tr_b16 a[218:219], %[rp] offset:288\n\t"
    "ds_write_b128 %[wa], a[220:223] offset:0\n\t"
    "ds_write_b128 %[wa], a[224:227] offset:1024\n\t"
    "ds_read_b64_tr_b16 a[220:221], %[rp] offset:0\n\t"
    "ds_read_b64_tr_b16 a[222:223], %[rp] offset:256\n\t"
    "ds_read_b64_tr_b16 a[224:225], %[rp] offset:32\n\t"
    "ds_read_b64_tr_b16 a[226:227], %[rp] offset:288\n\t"
    "ds_write_b128 %[wa], a[228:231] offset:0\n\t"
    "ds_write_b128 %[wa], a[232:235] offset:1024\n\t"
    "ds_read_b64_tr_b16 a[228:229], %[rp] offset:0\n\t"
    "ds_read_b64_tr_b16 a[230:231], %[rp] offset:256\n\t"
    "ds_read_b64_tr_b16 a[232:233], %[rp] offset:32\n\t"
    "ds_read_b64_tr_b16 a[234:235], %[rp] offset:288\n\t"
    "ds_write_b128 %[wa], a[236:239] offset:0\n\t"
    "ds_read_b64_tr_b16 a[236:237], %[rs] offset:0\n\t"
    "ds_read_b64_tr_b16 a[238:239], %[rs] offset:32\n\t"
    "ds_write_b128 %[wa], a[240:243] offset:0\n\t"
    "ds_read_b64_tr_b16 a[240:241], %[rs] offset:0\n\t"
    "ds_read_b64_tr_b16 a[242:243], %[rs] offset:32\n\t"
    "ds_write_b128 %[wa], a[244:247] offset:0\n\t"
    "ds_read_b64_tr_b16 a[244:245], %[rs] offset:0\n\t"
    "ds_read_b64_tr_b16 a[246:247], %[rs] offset:32\n\t"
    "ds_write_b128 %[wa], a[248:251] offset:0\n\t"
    "ds_read_b64_tr_b16 a[248:249], %[rs] offset:0\n\t"
    "ds_read_b64_tr_b16 a[250:251], %[rs] offset:32\n\t"
    "ds_write_b128 %[wa], a[252:255] offset:0\n\t"
    "ds_read_b64_tr_b16 a[252:253], %[rs] offset:0\n\t"
    "ds_read_b64_tr_b16 a[254:255], %[rs] offset:32\n\t"
    "s_waitcnt lgkmcnt(0)\n\t"
          :: [wa] "v"(wa), [rp] "v"(rp), [rs] "v"(rq) : "memory");
      }
      if (!(LATE_NOBAR && late && st == 1)) asm volatile("s_barrier" ::: "memory");
    }
    if (layer == 0) {
      const int b = (TAB_B >> (4 * n0)) & 3;
      gather_quarter(rs, X_OFF + 2560, (n0 + 1) & 3, (n0 + 1) >> 2, w, HB_OFF + 8 * w + 96, HB_OFF + 8 * w + 100);
      STAMP(12 + 6 * n0);
      BAR();
      float o0, o1, o2;
      { LANE();
        const unsigned ya = YS_OFF + 1280 * (j & 1) + 16 * c, ia = WQI_OFF + 6144 * w + 16 * lane;
        asm volatile(
    "ds_read_b128 v[16:19], %[ia] offset:0\n\t"
    "ds_read_b128 v[20:23], %[ia] offset:1024\n\t"
    "ds_read_b128 v[24:27], %[ia] offset:2048\n\t"
    "ds_read_b128 v[28:31], %[ya] offset:1152\n\t"
    "ds_read_b128 v[32:35], %[ya] offset:0\n\t"
    "ds_read_b128 v[36:39], %[ya] offset:64\n\t"
    "s_waitcnt lgkmcnt(2)\n\t"
    "v_mfma_f32_16x16x32_bf16 v[40:43], v[28:31], v[16:19], 0\n\t"
    "v_mfma_f32_16x16x32_bf16 v[44:47], v[28:31], v[20:23], 0\n\t"
    "v_mfma_f32_16x16x32_bf16 v[48:51], v[28:31], v[24:27], 0\n\t"
    "ds_read_b128 v[16:19], %[ia] offset:3072\n\t"
    "ds_read_b128 v[20:23], %[ia] offset:4096\n\t"
    "ds_read_b128 v[24:27], %[ia] offset:5120\n\t"
    "ds_read_b128 v[28:31], %[ya] offset:1216\n\t"
    "s_waitcnt lgkmcnt(5)\n\t"
    "v_mfma_f32_16x16x32_bf16 v[40:43], v[32:35], v[56:59], v[40:43]\n\t"
    "v_mfma_f32_16x16x32_bf16 v[44:47], v[32:35], v[60:63], v[44:47]\n\t"
    "v_mfma_f32_16x16x32_bf16 v[48:51], v[32:35], v[64:67], v[48:51]\n\t"
    "s_waitcnt lgkmcnt(4)\n\t"
    "v_mfma_f32_16x16x32_bf16 v[40:43], v[36:39], v[68:71], v[40:43]\n\t"
    "v_mfma_f32_16x16x32_bf16 v[44:47], v[36:39], v[72:75], v[44:47]\n\t"
    "v_mfma_f32_16x16x32_bf16 v[48:51], v[36:39], v[76:79], v[48:51]\n\t"
    "ds_read_b128 v[32:35], %[ya] offset:128\n\t"
    "ds_read_b128 v[36:39], %[ya] offset:192\n\t"
    "s_waitcnt lgkmcnt(2)\n\t"
    "v_mfma_f32_16x16x32_bf16 v[40:43], v[28:31], v[16:19], v[40:43]\n\t"
    "v_mfma_f32_16x16x32_bf16 v[44:47], v[28:31], v[20:23], v[44:47]\n\t"
    "v_mfma_f32_16x16x32_bf16 v[48:51], v[28:31], v[24:27], v[48:51]\n\t"
    "ds_read_b128 v[16:19], %[ya] offset:256\n\t"
    "ds_read_b128 v[20:23], %[ya] offset:320\n\t"
    "ds_read_b128 v[24:27], %[ya] offset:384\n\t"
    "ds_read_b128 v[28:31], %[ya] offset:448\n\t"
    "s_waitcnt lgkmcnt(5)\n\t"
    "v_mfma_f32_16x16x32_bf16 v[40:43], v[32:35], v[80:83], v[40:43]\n\t"
    "v_mfma_f32_16x16x32_bf16 v[44:47], v[32:35], v[84:87], v[44:47]\n\t"
    "v_mfma_f32_16x16x32_bf16 v[48:51], v[32:35], v[88:91], v[48:51]\n\t"
    "ds_read_b128 v[32:35], %[ya] offset:512\n\t"
    "s_waitcnt lgkmcnt(5)\n\t"
    "v_mfma_f32_16x16x32_bf16 v[40:43], v[36:39], v[92:95], v[40:43]\n\t"
    "v_mfma_f32_16x16x32_bf16 v[44:47], v[36:39], v[96:99], v[44:47]\n\t"
    "v_mfma_f32_16x16x32_bf16 v[48:51], v[36:39], v[100:103], v[48:51]\n\t"
    "ds_read_b128 v[36:39], %[ya] offset:576\n\t"
    "s_waitcnt lgkmcnt(5)\n\t"
    "v_mfma_f32_16x16x32_bf16 v[40:43], v[16:19], v[104:107], v[40:43]\n\t"
    "v_mfma_f32_16x16x32_bf16 v[44:47], v[16:19], v[108:111], v[44:47]\n\t"
    "v_mfma_f32_16x16x32_bf16 v[48:51], v[16:19], v[112:115], v[48:51]\n\t"
    "ds_read_b128 v[16:19], %[ya] offset:640\n\t"
    "s_waitcnt lgkmcnt(5)\n\t"
    "v_mfma_f32_16x16x32_bf16 v[40:43], v[20:23], v[116:119], v[40:43]\n\t"
    "v_mfma_f32_16x16x32_bf16 v[44:47], v[20:23], v[120:123], v[44:47]\n\t"
    "v_mfma_f32_16x16x32_bf16 v[48:51], v[20:23], v[124:127], v[48:51]\n\t"
    "ds_read_b128 v[20:23], %[ya] offset:704\n\t"
    "s_waitcnt lgkmcnt(5)\n\t"
    "v_mfma_f32_16x16x32_bf16 v[40:43], v[24:27], v[128:131], v[40:43]\n\t"
    "v_mfma_f32_16x16x32_bf16 v[44:47], v[24:27], v[132:135], v[44:47]\n\t"
    "v_mfma_f32_16x16x32_bf16 v[48:51], v[24:27], v[136:139], v[48:51]\n\t"
    "ds_read_b128 v[24:27], %[ya] offset:768\n\t"
    "s_waitcnt lgkmcnt(5)\n\t"
    "v_mfma_f32_16x16x32_bf16 v[40:43], v[28:31], v[140:143], v[40:43]\n\t"
    "v_mfma_f32_16x16x32_bf16 v[44:47], v[28:31], v[144:147], v[44:47]\n\t"
    "v_mfma_f32_16x16x32_bf16 v[48:51], v[28:31], v[148:151], v[48:51]\n\t"
    "ds_read_b128 v[28:31], %[ya] offset:832\n\t"
    "s_waitcnt lgkmcnt(5)\n\t"
    "v_mfma_f32_16x16x32_bf16 v[40:43], v[32:35], v[152:155], v[40:43]\n\t"
    "v_mfma_f32_16x16x32_bf16 v[44:47], v[32:35], v[156:159], v[44:47]\n\t"
    "v_mfma_f32_16x16x32_bf16 v[48:51], v[32:35], v[160:163], v[48:51]\n\t"
    "ds_read_b128 v[32:35], %[ya] offset:896\n\t"
    "s_waitcnt lgkmcnt(5)\n\t"
    "v_mfma_f32_16x16x32_bf16 v[40:43], v[36:39], v[164:167], v[40:43]\n\t"
    "v_mfma_f32_16x16x32_bf16 v[44:47], v[36:39], v[168:171], v[44:47]\n\t"
    "v_mfma_f32_16x16x32_bf16 v[48:51], v[36:39], v[172:175], v[48:51]\n\t"
    "ds_read_b128 v[36:39], %[ya] offset:960\n\t"
    "s_waitcnt lgkmcnt(5)\n\t"
    "v_mfma_f32_16x16x32_bf16 v[40:43], v[16:19], v[176:179], v[40:43]\n\t"
    "v_mfma_f32_16x16x32_bf16 v[44:47], v[16:19], v[180:183], v[44:47]\n\t"
    "v_mfma_f32_16x16x32_bf16 v[48:51], v[16:19], v[184:187], v[48:51]\n\t"
    "ds_read_b128 v[16:19], %[ya] offset:1024\n\t"
    "s_waitcnt lgkmcnt(5)\n\t"
    "v_mfma_f32_16x16x32_bf16 v[40:43], v[20:23], v[188:191], v[40:43]\n\t"
    "v_mfma_f32_16x16x32_bf16 v[44:47], v[20:23], v[192:195], v[44:47]\n\t"
    "v_mfma_f32_16x16x32_bf16 v[48:51], v[20:23], v[196:199], v[48:51]\n\t"
    "ds_read_b128 v[20:23], %[ya] offset:1088\n\t"
    "s_waitcnt lgkmcnt(5)\n\t"
    "v_mfma_f32_16x16x32_bf16 v[40:43], v[24:27], v[200:203], v[40:43]\n\t"
    "v_mfma_f32_16x16x32_bf16 v[44:47], v[24:27], v[204:207], v[44:47]\n\t"
    "v_mfma_f32_16x16x32_bf16 v[48:51], v[24:27], v[208:211], v[48:51]\n\t"
    "s_waitcnt lgkmcnt(4)\n\t"
    "v_mfma_f32_16x16x32_bf16 v[40:43], v[28:31], v[212:215], v[40:43]\n\t"
    "v_mfma_f32_16x16x32_bf16 v[44:47], v[28:31], v[216:219], v[44:47]\n\t"
    "v_mfma_f32_16x16x32_bf16 v[48:51], v[28:31], v[220:223], v[48:51]\n\t"
    "s_waitcnt lgkmcnt(3)\n\t"
    "v_mfma_f32_16x16x32_bf16 v[40:43], v[32:35], v[224:227], v[40:43]\n\t"
    "v_mfma_f32_16x16x32_bf16 v[44:47], v[32:35], v[228:231], v[44:47]\n\t"
    "v_mfma_f32_16x16x32_bf16 v[48:51], v[32:35], v[232:235], v[48:51]\n\t"
    "s_waitcnt lgkmcnt(2)\n\t"
    "v_mfma_f32_16x16x32_bf16 v[40:43], v[36:39], v[236:239], v[40:43]\n\t"
    "v_mfma_f32_16x16x32_bf16 v[44:47], v[36:39], v[240:243], v[44:47]\n\t"
    "v_mfma_f32_16x16x32_bf16 v[48:51], v[36:39], v[244:247], v[48:51]\n\t"
    "s_waitcnt lgkmcnt(1)\n\t"
    "v_mfma_f32_16x16x32_bf16 v[40:43], v[16:19], v[248:251], v[40:43]\n\t"
    "v_mfma_f32_16x16x32_bf16 v[44:47], v[16:19], v[252:255], v[44:47]\n\t"
    "v_mfma_f32_16x16x32_bf16 v[48:51], v[16:19], a[0:3], v[48:51]\n\t"
    "s_waitcnt lgkmcnt(0)\n\t"
    "v_mfma_f32_16x16x32_bf16 v[40:43], v[20:23], a[4:7], v[40:43]\n\t"
    "v_mfma_f32_16x16x32_bf16 v[44:47], v[20:23], a[8:11], v[44:47]\n\t"
    "v_mfma_f32_16x16x32_bf16 v[48:51], v[20:23], a[12:15], v[48:51]\n\t"
    "s_nop 7\n\t"
    "v_add_f32 %[o0], v40, v41\n\t"
    "v_add_f32 %[o1], v44, v45\n\t"
    "v_add_f32 %[o2], v48, v49\n\t"
        : [o0] "=&v"(o0), [o1] "=&v"(o1), [o2] "=&v"(o2) : [ya] "v"(ya), [ia] "v"(ia) : "memory", TCLOB); }
      LANE();
      const v4u sa = *(LAS v4u*)(uintptr_t)(HB_OFF + 96), sb = *(LAS v4u*)(uintptr_t)(HB_OFF + 112);
      const float mean = ((__uint_as_float(sa.x) + __uint_as_float(sa.z)) + (__uint_as_float(sb.x) + __uint_as_float(sb.z))) * (1.0f / 640.0f);
      const float ex2 = ((__uint_as_float(sa.y) + __uint_as_float(sa.w)) + (__uint_as_float(sb.y) + __uint_as_float(sb.w))) * (1.0f / 640.0f);
      const float rq = __builtin_amdgcn_rsqf(fmaf(fmaxf(fmaf(-mean, mean, ex2), 0.f), 1.0f / (QS * QS), 1e-5f / (QS * QS)));
      o0 = (o0 - mean * lds_rf(SQ_OFF + 192 * w + 4 * j)) * rq; o1 = (o1 - mean * lds_rf(SQ_OFF + 192 * w + 64 + 4 * j)) * rq; o2 = (o2 - mean * lds_rf(SQ_OFF + 192 * w + 128 + 4 * j)) * rq;
      if (c == 0) __builtin_amdgcn_raw_buffer_store_b64(v2u{pk_bf16(o0, o1), (pk_bf16(o2, 0.f) & 0xffffu) | 0x10000u}, rs, Q_OFF + h * 4096 + (4 + b) * 512 + w * 128 + j * 8, 0, SC1);
    }
    STAMP(13 + 6 * n0);
    if (!late) {
      LANE();
      unsigned so;
      const unsigned gp = (lane >> 2) * 1280 + (lane & 3) * 16, gq = (lane >> 1) * 1280 + (lane & 1) * 16, sb = h * 192 * 1280 + 320 * w;
      const unsigned wa = xs + 16 * lane, rp = xs + (8 * c + (j >> 2)) * 64 + 8 * (j & 3), rt = xs + (8 * c + (j >> 2)) * 32 + 8 * (j & 3);
      if (n0 < N_LASTQ) { asm volatile(
    "s_add_u32 %[so], %[sb], 0\n\t"
    "buffer_load_dwordx4 a[196:199], %[gp], %[srd], %[so] offen offset:0\n\t"
    "s_add_u32 %[so], %[sb], 20480\n\t"
    "buffer_load_dwordx4 a[200:203], %[gp], %[srd], %[so] offen offset:0\n\t"
    "s_add_u32 %[so], %[sb], 40960\n\t"
    "buffer_load_dwordx4 a[204:207], %[gp], %[srd], %[so] offen offset:0\n\t"
    "s_add_u32 %[so], %[sb], 61440\n\t"
    "buffer_load_dwordx4 a[208:211], %[gp], %[srd], %[so] offen offset:0\n\t"
    "s_add_u32 %[so], %[sb], 81920\n\t"
    "buffer_load_dwordx4 a[212:215], %[gp], %[srd], %[so] offen offset:0\n\t"
    "s_add_u32 %[so], %[sb], 102400\n\t"
    "buffer_load_dwordx4 a[216:219], %[gp], %[srd], %[so] offen offset:0\n\t"
    "s_add_u32 %[so], %[sb], 122880\n\t"
    "buffer_load_dwordx4 a[220:223], %[gp], %[srd], %[so] offen offset:0\n\t"
    "s_add_u32 %[so], %[sb], 143360\n\t"
    "buffer_load_dwordx4 a[224:227], %[gp], %[srd], %[so] offen offset:0\n\t"
    "s_add_u32 %[so], %[sb], 163840\n\t"
    "buffer_load_dwordx4 a[228:231], %[gp], %[srd], %[so] offen offset:0\n\t"
    "s_add_u32 %[so], %[sb], 184320\n\t"
    "buffer_load_dwordx4 a[232:235], %[gp], %[srd], %[so] offen offset:0\n\t"
    "s_add_u32 %[so], %[sb], 204800\n\t"
    "buffer_load_dwordx4 a[236:239], %[gp], %[srd], %[so] offen offset:0\n\t"
    "s_add_u32 %[so], %[sb], 225280\n\t"
    "buffer_load_dwordx4 a[240:243], %[gp], %[srd], %[so] offen offset:0\n\t"
    "s_add_u32 %[so], %[sb], 0\n\t"
    "buffer_load_dwordx4 a[244:247], %[gq], %[srd], %[so] offen offset:64\n\t"
    "s_add_u32 %[so], %[sb], 40960\n\t"
    "buffer_load_dwordx4 a[248:251], %[gq], %[srd], %[so] offen offset:64\n\t"
    "s_add_u32 %[so], %[sb], 81920\n\t"
    "buffer_load_dwordx4 a[252:255], %[gq], %[srd], %[so] offen offset:64\n\t"
    "s_waitcnt vmcnt(0)\n\t"
    "ds_write_b128 %[wa], a[196:199] offset:0\n\t"
    "ds_write_b128 %[wa], a[200:203] offset:1024\n\t"
    "ds_read_b64_tr_b16 a[196:197], %[rp] offset:0\n\t"
    "ds_read_b64_tr_b16 a[198:199], %[rp] offset:256\n\t"
    "ds_read_b64_tr_b16 a[200:201], %[rp] offset:32\n\t"
    "ds_read_b64_tr_b16 a[202:203], %[rp] offset:288\n\t"
    "ds_write_b128 %[wa], a[204:207] offset:0\n\t"
    "ds_write_b128 %[wa], a[208:211] offset:1024\n\t"
    "ds_read_b64_tr_b16 a[204:205], %[rp] offset:0\n\t"
    "ds_read_b64_tr_b16 a[206:207], %[rp] offset:256\n\t"
    "ds_read_b64_tr_b16 a[208:209], %[rp] offset:32\n\t"
    "ds_read_b64_tr_b16 a[210:211], %[rp] offset:288\n\t"
    "ds_write_b128 %[wa], a[212:215] offset:0\n\t"
    "ds_write_b128 %[wa], a[216:219] offset:1024\n\t"
    "ds_read_b64_tr_b16 a[212:213], %[rp] offset:0\n\t"
    "ds_read_b64_tr_b16 a[214:215], %[rp] offset:256\n\t"
    "ds_read_b64_tr_b16 a[216:217], %[rp] offset:32\n\t"
    "ds_read_b64_tr_b16 a[218:219], %[rp] offset:288\n\t"
    "ds_write_b128 %[wa], a[220:223] offset:0\n\t"
    "ds_write_b128 %[wa], a[224:227] offset:1024\n\t"
    "ds_read_b64_tr_b16 a[220:221], %[rp] offset:0\n\t"
    "ds_read_b64_tr_b16 a[222:223], %[rp] offset:256\n\t"
    "ds_read_b64_tr_b16 a[224:225], %[rp] offset:32\n\t"
    "ds_read_b64_tr_b16 a[226:227], %[rp] offset:288\n\t"
    "ds_write_b128 %[wa], a[228:231] offset:0\n\t"
    "ds_write_b128 %[wa], a[232:235] offset:1024\n\t"
    "ds_read_b64_tr_b16 a[228:229], %[rp] offset:0\n\t"
    "ds_read_b64_tr_b16 a[230:231], %[rp] offset:256\n\t"
    "ds_read_b64_tr_b16 a[232:233], %[rp] offset:32\n\t"
    "ds_read_b64_tr_b16 a[234:235], %[rp] offset:288\n\t"
    "ds_write_b128 %[wa], a[236:239] offset:0\n\t"
    "ds_write_b128 %[wa], a[240:243] offset:1024\n\t"
    "ds_read_b64_tr_b16 a[236:237], %[rp] offset:0\n\t"
    "ds_read_b64_tr_b16 a[238:239], %[rp] offset:256\n\t"
    "ds_read_b64_tr_b16 a[240:241], %[rp] offset:32\n\t"
    "ds_read_b64_tr_b16 a[242:243], %[rp] offset:288\n\t"
    "ds_write_b128 %[wa], a[244:247] offset:0\n\t"
    "ds_read_b64_tr_b16 a[244:245], %[rt] offset:0\n\t"
    "ds_read_b64_tr_b16 a[246:247], %[rt] offset:128\n\t"
    "ds_write_b128 %[wa], a[248:251] offset:0\n\t"
    "ds_read_b64_tr_b16 a[248:249], %[rt] offset:0\n\t"
    "ds_read_b64_tr_b16 a[250:251], %[rt] offset:128\n\t"
    "ds_write_b128 %[wa], a[252:255] offset:0\n\t"
    "ds_read_b64_tr_b16 a[252:253], %[rt] offset:0\n\t"
    "ds_read_b64_tr_b16 a[254:255], %[rt] offset:128\n\t"
    "s_waitcnt lgkmcnt(0)\n\t"
        : [so] "=&s"(so) : [gp] "v"(gp), [gq] "v"(gq), [srd] "s"(srd_wo), [sb] "s"(sb), [wa] "v"(wa), [rp] "v"(rp), [rt] "v"(rt) : "memory", "scc"); }
      else { asm volatile(
    "s_add_u32 %[so], %[sb], 0\n\t"
    "buffer_load_dwordx4 v[56:59], %[gp], %[srd], %[so] offen offset:0\n\t"
    "s_add_u32 %[so], %[sb], 20480\n\t"
    "buffer_load_dwordx4 v[60:63], %[gp], %[srd], %[so] offen offset:0\n\t"
    "s_add_u32 %[so], %[sb], 40960\n\t"
    "buffer_load_dwordx4 v[64:67], %[gp], %[srd], %[so] offen offset:0\n\t"
    "s_add_u32 %[so], %[sb], 61440\n\t"
    "buffer_load_dwordx4 v[68:71], %[gp], %[srd], %[so] offen offset:0\n\t"
    "s_add_u32 %[so], %[sb], 81920\n\t"
    "buffer_load_dwordx4 v[72:75], %[gp], %[srd], %[so] offen offset:0\n\t"
    "s_add_u32 %[so], %[sb], 102400\n\t"
    "buffer_load_dwordx4 v[76:79], %[gp], %[srd], %[so] offen offset:0\n\t"
    "s_add_u32 %[so], %[sb], 122880\n\t"
    "buffer_load_dwordx4 v[80:83], %[gp], %[srd], %[so] offen offset:0\n\t"
    "s_add_u32 %[so], %[sb], 143360\n\t"
    "buffer_load_dwordx4 v[84:87], %[gp], %[srd], %[so] offen offset:0\n\t"
    "s_add_u32 %[so], %[sb], 163840\n\t"
    "buffer_load_dwordx4 v[88:91], %[gp], %[srd], %[so] offen offset:0\n\t"
    "s_add_u32 %[so], %[sb], 184320\n\t"
    "buffer_load_dwordx4 v[92:95], %[gp], %[srd], %[so] offen offset:0\n\t"
    "s_add_u32 %[so], %[sb], 204800\n\t"
    "buffer_load_dwordx4 v[96:99], %[gp], %[srd], %[so] offen offset:0\n\t"
    "s_add_u32 %[so], %[sb], 225280\n\t"
    "buffer_load_dwordx4 v[100:103], %[gp], %[srd], %[so] offen offset:0\n\t"
    "s_add_u32 %[so], %[sb], 0\n\t"
    "buffer_load_dwordx4 v[104:107], %[gq], %[srd], %[so] offen offset:64\n\t"
    "s_add_u32 %[so], %[sb], 40960\n\t"
    "buffer_load_dwordx4 v[108:111], %[gq], %[srd], %[so] offen offset:64\n\t"
    "s_add_u32 %[so], %[sb], 81920\n\t"
    "buffer_load_dwordx4 v[112:115], %[gq], %[srd], %[so] offen offset:64\n\t"
    "s_waitcnt vmcnt(0)\n\t"
    "ds_write_b128 %[wa], v[56:59] offset:0\n\t"
    "ds_write_b128 %[wa], v[60:63] offset:1024\n\t"
    "ds_read_b64_tr_b16 v[56:57], %[rp] offset:0\n\t"
    "ds_read_b64_tr_b16 v[58:59], %[rp] offset:256\n\t"
    "ds_read_b64_tr_b16 v[60:61], %[rp] offset:32\n\t"
    "ds_read_b64_tr_b16 v[62:63], %[rp] offset:288\n\t"
    "ds_write_b128 %[wa], v[64:67] offset:0\n\t"
    "ds_write_b128 %[wa], v[68:71] offset:1024\n\t"
    "ds_read_b64_tr_b16 v[64:65], %[rp] offset:0\n\t"
    "ds_read_b64_tr_b16 v[66:67], %[rp] offset:256\n\t"
    "ds_read_b64_tr_b16 v[68:69], %[rp] offset:32\n\t"
    "ds_read_b64_tr_b16 v[70:71], %[rp] offset:288\n\t"
    "ds_write_b128 %[wa], v[72:75] offset:0\n\t"
    "ds_write_b128 %[wa], v[76:79] offset:1024\n\t"
    "ds_read_b64_tr_b16 v[72:73], %[rp] offset:0\n\t"
    "ds_read_b64_tr_b16 v[74:75], %[rp] offset:256\n\t"
    "ds_read_b64_tr_b16 v[76:77], %[rp] offset:32\n\t"
    "ds_read_b64_tr_b16 v[78:79], %[rp] offset:288\n\t"
    "ds_write_b128 %[wa], v[80:83] offset:0\n\t"
    "ds_write_b128 %[wa], v[84:87] offset:1024\n\t"
    "ds_read_b64_tr_b16 v[80:81], %[rp] offset:0\n\t"
    "ds_read_b64_tr_b16 v[82:83], %[rp] offset:256\n\t"
    "ds_read_b64_tr_b16 v[84:85], %[rp] offset:32\n\t"
    "ds_read_b64_tr_b16 v[86:87], %[rp] offset:288\n\t"
    "ds_write_b128 %[wa], v[88:91] offset:0\n\t"
    "ds_write_b128 %[wa], v[92:95] offset:1024\n\t"
    "ds_read_b64_tr_b16 v[88:89], %[rp] offset:0\n\t"
    "ds_read_b64_tr_b16 v[90:91], %[rp] offset:256\n\t"
    "ds_read_b64_tr_b16 v[92:93], %[rp] offset:32\n\t"
    "ds_read_b64_tr_b16 v[94:95], %[rp] offset:288\n\t"
    "ds_write_b128 %[wa], v[96:99] offset:0\n\t"
    "ds_write_b128 %[wa], v[100:103] offset:1024\n\t"
    "ds_read_b64_tr_b16 v[96:97], %[rp] offset:0\n\t"
    "ds_read_b64_tr_b16 v[98:99], %[rp] offset:256\n\t"
    "ds_read_b64_tr_b16 v[100:101], %[rp] offset:32\n\t"
    "ds_read_b64_tr_b16 v[102:103], %[rp] offset:288\n\t"
    "ds_write_b128 %[wa], v[104:107] offset:0\n\t"
    "ds_read_b64_tr_b16 v[104:105], %[rt] offset:0\n\t"
    "ds_read_b64_tr_b16 v[106:107], %[rt] offset:128\n\t"
    "ds_write_b128 %[wa], v[108:111] offset:0\n\t"
    "ds_read_b64_tr_b16 v[108:109], %[rt] offset:0\n\t"
    "ds_read_b64_tr_b16 v[110:111], %[rt] offset:128\n\t"
    "ds_write_b128 %[wa], v[112:115] offset:0\n\t"
    "ds_read_b64_tr_b16 v[112:113], %[rt] offset:0\n\t"
    "ds_read_b64_tr_b16 v[114:115], %[rt] offset:128\n\t"
    "s_waitcnt lgkmcnt(0)\n\t"
        "s_barrier"
        : [so] "=&s"(so) : [gp] "v"(gp), [gq] "v"(gq), [srd] "s"(srd_wo), [sb] "s"(sb), [wa] "v"(wa), [rp] "v"(rp), [rt] "v"(rt) : "memory", "scc"); }
    }
    n0 += nr;
  }
  asm volatile(".Lch_end:");
  if (h == 0 && w == 0 && lane_id() == 0) __builtin_amdgcn_raw_buffer_store_b64(v2u{(unsigned)ep, (unsigned)(ep >> 32)}, rs, 0, 0, SC1);
}

extern "C" void kernel_launch(void* const* d_in, const int* in_sizes, int n_in, void* d_out, int out_size, void* d_ws, size_t ws_size, hipStream_t stream) {
  (void)in_sizes; (void)n_in; (void)out_size; (void)ws_size;
  (void)hipFuncSetAttribute((const void*)fused_decode, hipFuncAttributeMaxDynamicSharedMemorySize, LDS_BYTES);
  hipLaunchKernelGGL(fused_decode, dim3(256), dim3(256), LDS_BYTES, stream,
                     (const unsigned short*)d_in[0], (const unsigned short*)d_in[1], (const unsigned short*)d_in[2], (const unsigned short*)d_in[3],
                     (const unsigned short*)d_in[4], (const unsigned short*)d_in[6], (const unsigned short*)d_in[7], (const unsigned short*)d_in[8],
                     (unsigned short*)d_out, (unsigned char*)d_ws);
}
```
